```python
import torch

def _softmax(x, axis=-1):
    return torch.softmax(x, dim=axis)
def _gelu(x):
    return torch.nn.functional.gelu(x, approximate="tanh")
def _silu(x):
    return torch.nn.functional.silu(x)
def _sigmoid(x):
    return torch.sigmoid(x)
def _layernorm(x, axis=-1, eps=1e-5):
    m = x.mean(dim=axis, keepdim=True)
    v = x.var(dim=axis, keepdim=True, unbiased=False)
    return (x - m) / torch.sqrt(v + eps)
def _scatter_add(tgt, idx, src, axis=0):
    return tgt.clone().index_add_(axis, idx, src)
def _segment_sum(src, seg, n):
    out = torch.zeros((n,) + src.shape[1:], dtype=src.dtype)
    return out.index_add_(0, seg, src)
def _pad(x, pw):
    flat = tuple(v for pair in reversed(pw) for v in pair)
    return torch.nn.functional.pad(x, flat)

kernel_name = 'gated_mix__compressed_kv_cla__swiglu_ffn__decode'

def setup_inputs():

    _w = (lambda a: (lambda t: t.to(torch.bfloat16) if t.is_floating_point() else t)(torch.as_tensor(a)))
    def _A_setup():
        B, H, Sq, Sk, D, M = 2, 32, 1, 4096, 128, 640
        Dc = 4 * D
        return {
            "x": _w((lambda *s: torch.randn(*s, dtype=torch.bfloat16) * 0.05)(B, Sq, M)),
            "c_kv": _w((lambda *s: torch.randn(*s, dtype=torch.bfloat16) * 0.8)(B, Sk, Dc)),
            "w_q": _w((lambda *s: torch.randn(*s, dtype=torch.bfloat16) * 6.4)(M, H, D) * (M ** -0.5)),
            "w_uk": _w((lambda *s: torch.randn(*s, dtype=torch.bfloat16) * 6.4)(Dc, H, D) * (Dc ** -0.5)),
            "w_uv": _w((lambda *s: torch.randn(*s, dtype=torch.bfloat16) * 0.8)(Dc, H, D) * (Dc ** -0.5)),
            "w_o": _w((lambda *s: torch.randn(*s, dtype=torch.bfloat16) * 0.8)(H, D, M) * ((H * D) ** -0.5)),
        }
    _A_in = _A_setup()

    def _B_setup():
        B, S, D, F = 2, 1, 640, 1728
        return {
            "x": _w((lambda *s: torch.randn(*s, dtype=torch.bfloat16) * 0.05)(B, S, D)),
            "w_gate": _w((lambda *s: torch.randn(*s, dtype=torch.bfloat16) * 0.1)(D, F)),
            "w_up": _w((lambda *s: torch.randn(*s, dtype=torch.bfloat16) * 0.1)(D, F)),
            "w_down": _w((lambda *s: torch.randn(*s, dtype=torch.bfloat16) * 0.1)(F, D)),
        }
    _B_in = _B_setup()

    return {
        "A_x": _A_in["x"],
        "A_c_kv": _A_in["c_kv"],
        "A_w_q": _A_in["w_q"],
        "A_w_uk": _A_in["w_uk"],
        "A_w_uv": _A_in["w_uv"],
        "A_w_o": _A_in["w_o"],
        "B_x": _B_in["x"],
        "B_w_gate": _B_in["w_gate"],
        "B_w_up": _B_in["w_up"],
        "B_w_down": _B_in["w_down"],
        "gate_w": _w((lambda *s: torch.randn(*s, dtype=torch.bfloat16) * 0.1)(640) * 16.0),
    }

def reference(A_x, A_c_kv, A_w_q, A_w_uk, A_w_uv, A_w_o, B_x, B_w_gate, B_w_up, B_w_down, gate_w):
    def _A_ref(x, c_kv, w_q, w_uk, w_uv, w_o):
        B, Sq, M = x.shape[0], x.shape[1], x.shape[2]
        Sk = c_kv.shape[1]
        H, D = w_q.shape[1], w_q.shape[2]
        q = torch.einsum("bqm,mhd->bhqd", x, w_q)
        k = torch.einsum("bkc,chd->bhkd", c_kv, w_uk)
        v = torch.einsum("bkc,chd->bhkd", c_kv, w_uv)
        scores = torch.einsum("bhqd,bhkd->bhqk", q, k) * (D ** -0.5)
        off = Sk - Sq
        qi = torch.arange(Sq)[:, None]
        kj = torch.arange(Sk)[None, :]
        cmask = kj <= (qi + off)
        scores = torch.where(cmask[None, None], scores, -1e9)
        p = _softmax(scores, axis=-1)
        ctx = torch.einsum("bhqk,bhkd->bhqd", p, v)
        return torch.einsum("bhqd,hdm->bqm", ctx, w_o)

    def _B_ref(x, w_gate, w_up, w_down):
        g = _silu(x @ w_gate)
        u = x @ w_up
        return (g * u) @ w_down

    x = A_x
    g = _sigmoid((x * gate_w).sum(-1, keepdims=True))
    a = _A_ref(x, A_c_kv, A_w_q, A_w_uk, A_w_uv, A_w_o)
    b = _B_ref(x, B_w_gate, B_w_up, B_w_down)
    return g * a + (1 - g) * b
```

```cpp
#include <hip/hip_runtime.h>
#include <hip/hip_bf16.h>
#include <stdint.h>

typedef unsigned short u16;
__device__ __forceinline__ float bf2f(u16 v) { return __uint_as_float(((unsigned)v) << 16); }
__device__ __forceinline__ u16 f2bf(float f) { unsigned u = __float_as_uint(f); u += 0x7FFFu + ((u >> 16) & 1u); return (u16)(u >> 16); }
__device__ __forceinline__ float rbf(float f) { return bf2f(f2bf(f)); }

constexpr int B = 2, H = 32, SK = 4096, D = 128, M = 640, DC = 512, F = 1728;
constexpr int NS = 64;

__global__ void k1(const u16* x, const u16* wq, const u16* wg, const u16* wu, float* q, float* h1) {
  int n = blockIdx.x * blockDim.x + threadIdx.x;
  if (n < H * D) {
    float a0 = 0, a1 = 0;
    for (int m = 0; m < M; ++m) { float w = bf2f(wq[(size_t)m * H * D + n]); a0 += bf2f(x[m]) * w; a1 += bf2f(x[M + m]) * w; }
    q[n] = rbf(a0); q[H * D + n] = rbf(a1);
  } else if (n < H * D + F) {
    int f = n - H * D;
    float g0 = 0, g1 = 0, u0 = 0, u1 = 0;
    for (int m = 0; m < M; ++m) {
      float a = bf2f(wg[(size_t)m * F + f]), b = bf2f(wu[(size_t)m * F + f]);
      float x0 = bf2f(x[m]), x1 = bf2f(x[M + m]);
      g0 += x0 * a; g1 += x1 * a; u0 += x0 * b; u1 += x1 * b;
    }
    h1[f] = g0 / (1.f + __expf(-g0)) * u0;
    h1[F + f] = g1 / (1.f + __expf(-g1)) * u1;
  }
}
__global__ void k2(const float* q, const u16* wuk, float* qa) {
  int n = blockIdx.x * blockDim.x + threadIdx.x;
  int h = n / DC, c = n % DC;
  float a0 = 0, a1 = 0;
  for (int d = 0; d < D; ++d) { float w = bf2f(wuk[((size_t)c * H + h) * D + d]); a0 += q[h * D + d] * w; a1 += q[H * D + h * D + d] * w; }
  const float sc = 0.08838834764831845f;
  qa[n] = a0 * sc; qa[H * DC + n] = a1 * sc;
}
__global__ void k3(const float* qa, const u16* ckv, float* op, float* ml) {
  __shared__ float s[H][SK / NS];
  int b = blockIdx.x / NS, sp = blockIdx.x % NS;
  const int KP = SK / NS;
  const u16* cb = ckv + ((size_t)b * SK + sp * KP) * DC;
  for (int i = threadIdx.x; i < H * KP; i += blockDim.x) {
    int h = i / KP, k = i % KP;
    float a = 0;
    for (int c = 0; c < DC; ++c) a += qa[(b * H + h) * DC + c] * bf2f(cb[(size_t)k * DC + c]);
    s[h][k] = a;
  }
  __syncthreads();
  if (threadIdx.x < H) {
    int h = threadIdx.x;
    float mx = -1e30f;
    for (int k = 0; k < KP; ++k) mx = fmaxf(mx, s[h][k]);
    float l = 0;
    for (int k = 0; k < KP; ++k) { float p = __expf(s[h][k] - mx); s[h][k] = p; l += p; }
    ml[((b * NS + sp) * H + h) * 2] = mx; ml[((b * NS + sp) * H + h) * 2 + 1] = l;
  }
  __syncthreads();
  for (int i = threadIdx.x; i < H * DC; i += blockDim.x) {
    int h = i / DC, c = i % DC;
    float a = 0;
    for (int k = 0; k < KP; ++k) a += s[h][k] * bf2f(cb[(size_t)k * DC + c]);
    op[((size_t)(b * NS + sp) * H + h) * DC + c] = a;
  }
}
__global__ void k4(const float* op, const float* ml, const u16* wuv, float* ctx) {
  __shared__ float cc[DC];
  int b = blockIdx.x / H, h = blockIdx.x % H;
  float mx = -1e30f;
  for (int s = 0; s < NS; ++s) mx = fmaxf(mx, ml[((b * NS + s) * H + h) * 2]);
  float L = 0;
  for (int s = 0; s < NS; ++s) L += ml[((b * NS + s) * H + h) * 2 + 1] * __expf(ml[((b * NS + s) * H + h) * 2] - mx);
  for (int c = threadIdx.x; c < DC; c += blockDim.x) {
    float a = 0;
    for (int s = 0; s < NS; ++s) a += op[((size_t)(b * NS + s) * H + h) * DC + c] * __expf(ml[((b * NS + s) * H + h) * 2] - mx);
    cc[c] = a / L;
  }
  __syncthreads();
  for (int d = threadIdx.x; d < D; d += blockDim.x) {
    float a = 0;
    for (int c = 0; c < DC; ++c) a += cc[c] * bf2f(wuv[((size_t)c * H + h) * D + d]);
    ctx[(b * H + h) * D + d] = a;
  }
}
__global__ void k5(const u16* x, const u16* gw, const float* ctx, const float* h1, const u16* wo, const u16* wd, u16* out) {
  int n = blockIdx.x * blockDim.x + threadIdx.x;
  if (n >= B * M) return;
  int b = n / M, m = n % M;
  float gs = 0;
  for (int i = 0; i < M; ++i) gs += bf2f(x[b * M + i]) * bf2f(gw[i]);
  float g = 1.f / (1.f + __expf(-gs));
  float a = 0;
  for (int i = 0; i < H * D; ++i) a += ctx[b * H * D + i] * bf2f(wo[(size_t)i * M + m]);
  float f = 0;
  for (int i = 0; i < F; ++i) f += h1[b * F + i] * bf2f(wd[(size_t)i * M + m]);
  out[n] = f2bf(g * a + (1.f - g) * f);
}

extern "C" void kernel_launch(void* const* d_in, const int* in_sizes, int n_in,
                              void* d_out, int out_size, void* d_ws, size_t ws_size,
                              hipStream_t stream) {
  const u16* x = (const u16*)d_in[0];
  const u16* ckv = (const u16*)d_in[1];
  const u16* wq = (const u16*)d_in[2];
  const u16* wuk = (const u16*)d_in[3];
  const u16* wuv = (const u16*)d_in[4];
  const u16* wo = (const u16*)d_in[5];
  const u16* wg = (const u16*)d_in[7];
  const u16* wu = (const u16*)d_in[8];
  const u16* wd = (const u16*)d_in[9];
  const u16* gw = (const u16*)d_in[10];
  float* ws = (float*)d_ws;
  float* q = ws;
  float* h1 = q + 2 * 4096;
  float* qa = h1 + 4096;
  float* ml = qa + 2 * 32 * 512;
  float* ctx = ml + 2 * NS * 32 * 2;
  float* op = ctx + 2 * 4096;
  k1<<<(H * D + F + 63) / 64, 64, 0, stream>>>(x, wq, wg, wu, q, h1);
  k2<<<H * DC / 64, 64, 0, stream>>>(q, wuk, qa);
  k3<<<B * NS, 256, 0, stream>>>(qa, ckv, op, ml);
  k4<<<B * H, 128, 0, stream>>>(op, ml, wuv, ctx);
  k5<<<(B * M + 63) / 64, 64, 0, stream>>>(x, gw, ctx, h1, wo, wd, (u16*)d_out);
}
```

```cpp
#include <hip/hip_runtime.h>
#include <stdint.h>

#define STAMPS 0
#define F_DELAY 30
#define B_DELAY 0

typedef unsigned short u16;
typedef unsigned int u32;
typedef unsigned long long u64;
typedef u32 v4u __attribute__((ext_vector_type(4)));
typedef u32 v2u __attribute__((ext_vector_type(2)));
typedef float f4 __attribute__((ext_vector_type(4)));
typedef float f2 __attribute__((ext_vector_type(2)));
typedef float f16v __attribute__((ext_vector_type(16)));
typedef short bf8 __attribute__((ext_vector_type(8)));
typedef short s4 __attribute__((ext_vector_type(4)));
typedef __bf16 bf2 __attribute__((ext_vector_type(2)));
#define GAS __attribute__((address_space(1)))
#define LAS __attribute__((address_space(3)))
#define DI __device__ __forceinline__
typedef __amdgpu_buffer_rsrc_t rsrc_t;

constexpr int NB = 2, H = 32, SK = 4096, D = 128, M = 640, DC = 512, F = 1728;
constexpr int NT = 512;
constexpr int W_EPOCH = 0;
constexpr int W_H1 = 4096;
constexpr int W_QA = 65536;
constexpr int W_ML = 131072;
constexpr int W_FLAG = 196608;
constexpr int W_QP = 262144;
constexpr int W_PO = 524288;
constexpr int W_PF = 1179648;
constexpr int W_OP = 2097152;
constexpr int W_END = 4194304;
constexpr int W_DBG = 8388608;
constexpr int LDS_BYTES = 131072 + 1024;
constexpr int SC1 = 16, NTL = 2;
constexpr u32 SENT = 0xFFFFFFFFu;
constexpr u32 SPIN_CAP = 1u << 20;
constexpr int CODE_PF_BYTES = 16384;
constexpr float SCL = 0.08838834764831845f * 1.4426950408889634f;

struct Params {
  const u16* x; const u16* ckv; const u16* wq; const u16* wuk; const u16* wuv; const u16* wo;
  const u16* wg; const u16* wu; const u16* wd; const u16* gw; u16* out; unsigned char* ws;
};

DI rsrc_t mk(const void* p, int bytes) { return __builtin_amdgcn_make_buffer_rsrc((void*)p, 0, bytes, 0x00020000); }
DI v4u ldw(rsrc_t r, int voff, int soff) { return __builtin_amdgcn_raw_buffer_load_b128(r, voff, soff, NTL); }
DI v4u ldh(rsrc_t r, int voff) { return __builtin_amdgcn_raw_buffer_load_b128(r, voff, 0, SC1); }
DI v4u ldhs(rsrc_t r, int voff, int soff) { return __builtin_amdgcn_raw_buffer_load_b128(r, voff, soff, SC1); }
DI v2u ldh2(rsrc_t r, int voff) { return __builtin_amdgcn_raw_buffer_load_b64(r, voff, 0, SC1); }
DI void sth(v4u v, rsrc_t r, int voff) { __builtin_amdgcn_raw_buffer_store_b128(v, r, voff, 0, SC1); }
DI void sth2(v2u v, rsrc_t r, int voff) { __builtin_amdgcn_raw_buffer_store_b64(v, r, voff, 0, SC1); }
DI void sth1(u32 v, rsrc_t r, int voff) { __builtin_amdgcn_raw_buffer_store_b32(v, r, voff, 0, SC1); }
DI u32 umin(u32 a, u32 b) { return a < b ? a : b; }
DI u32 umax(u32 a, u32 b) { return a > b ? a : b; }
DI u32 guard(u32 v) { return umin(v, 0xFFFFFFFEu); }
DI v4u guard4(v4u v) { return v4u{guard(v.x), guard(v.y), guard(v.z), guard(v.w)}; }
DI u32 max4(v4u v) { return umax(umax(v.x, v.y), umax(v.z, v.w)); }
DI u32 pk2(float lo, float hi) { bf2 r = {(__bf16)lo, (__bf16)hi}; return __builtin_bit_cast(u32, r); }
DI float bflo(u32 v) { return __uint_as_float(v << 16); }
DI float bfhi(u32 v) { return __uint_as_float(v & 0xFFFF0000u); }
DI f4 mfma16(v4u a, v4u b, f4 c) { return __builtin_amdgcn_mfma_f32_16x16x32_bf16(__builtin_bit_cast(bf8, a), __builtin_bit_cast(bf8, b), c, 0, 0, 0); }
DI f16v mfma32(v4u a, v4u b, f16v c) { return __builtin_amdgcn_mfma_f32_32x32x16_bf16(__builtin_bit_cast(bf8, a), __builtin_bit_cast(bf8, b), c, 0, 0, 0); }
#define CBAR() asm volatile("" ::: "memory")
#define SB() __builtin_amdgcn_sched_barrier(0)
#define PIN(x) asm volatile("" : "+v"(x))
#define DRAIN() asm volatile("s_waitcnt vmcnt(0)" ::: "memory")
#define LBAR() do { asm volatile("s_waitcnt lgkmcnt(0)" ::: "memory"); __builtin_amdgcn_s_barrier(); CBAR(); } while (0)
DI float ror8(float v) { return __builtin_bit_cast(float, __builtin_amdgcn_update_dpp(0, __builtin_bit_cast(int, v), 0x128, 0xf, 0xf, false)); }
DI float swap16add(float x, float y) { auto t = __builtin_amdgcn_permlane16_swap(__float_as_uint(x), __float_as_uint(y), false, false); return __uint_as_float(t[0]) + __uint_as_float(t[1]); }
DI float swap32add(float x, float y) { auto t = __builtin_amdgcn_permlane32_swap(__float_as_uint(x), __float_as_uint(y), false, false); return __uint_as_float(t[0]) + __uint_as_float(t[1]); }
DI v4u selfrag(u32 vv, v4u mask) { return v4u{vv & mask.x, vv & mask.y, vv & mask.z, vv & mask.w}; }
DI u32 duplo(u32 v) { return __builtin_amdgcn_perm(v, v, 0x01000100u); }
DI u32 duphi(u32 v) { return __builtin_amdgcn_perm(v, v, 0x03020302u); }
DI int swz(int row) { return ((row & 3) << 2) | ((row >> 2) & 3); }
DI void dma(const u16* src, LAS unsigned char* dst) { __builtin_amdgcn_global_load_lds((const GAS u32*)src, (LAS u32*)dst, 16, 0, NTL); }
DI v4u trfrag(LAS unsigned char* a0, LAS unsigned char* a1) {
  const v2u lo = __builtin_bit_cast(v2u, __builtin_amdgcn_ds_read_tr16_b64_v4i16((LAS s4*)a0));
  const v2u hi = __builtin_bit_cast(v2u, __builtin_amdgcn_ds_read_tr16_b64_v4i16((LAS s4*)a1));
  return v4u{lo.x, lo.y, hi.x, hi.y};
}

#define ROLE_OF(b) ((b) < 128 ? 0 : (b) < 192 ? 1 : ((b) < 246 ? 2 : -1))
#define STAMP(k) do {} while (0)

DI void wait_flags(rsrc_t rw, int idx, v2u tag) {
#pragma unroll 1
  for (u32 sp = 0; sp < SPIN_CAP; ++sp) {
    CBAR();
    v2u f = ldh2(rw, W_FLAG + idx * 128);
    if (!__any((f.x != tag.x) | (f.y != tag.y))) break;
    __builtin_amdgcn_s_sleep(1);
  }
  CBAR();
}

__global__ void __launch_bounds__(NT, 2) mk_kernel(Params p) {
  extern __shared__ __attribute__((aligned(16))) unsigned char smem[];
  LAS unsigned char* L = (LAS unsigned char*)smem;
  const int tid = threadIdx.x, l = tid & 63, w = __builtin_amdgcn_readfirstlane(tid >> 6);
  const int bid = blockIdx.x;
  const int r = l & 15, g = l >> 4;
  STAMP(0);
  u64 pc0; asm volatile("s_getpc_b64 %0" : "=s"(pc0));
  u32 cpf = 0;
  if (w >= 6) cpf = ((const GAS u32*)(pc0 & ~127ull))[umin(tid - 384, CODE_PF_BYTES / 128 - 1) * 32];
  CBAR(); SB();
  const rsrc_t rw = mk(p.ws, W_END);
  const rsrc_t rx = mk(p.x, NB * M * 2);
  const v4u sent4 = {SENT, SENT, SENT, SENT};
  const u32 m16 = (l & 1) ? 0xFFFF0000u : 0x0000FFFFu;
  const int jq = (l & 7) >> 1;
  const v4u mask = {jq == 0 ? m16 : 0u, jq == 1 ? m16 : 0u, jq == 2 ? m16 : 0u, jq == 3 ? m16 : 0u};
  const int bsel = (l >> 3) & 1;

  if (__builtin_expect(bid < 128, 1)) {
    const int h = (bid & 7) * 4 + (bid >> 5), j = (bid >> 3) & 3, ac = h * 4 + j;
    u32 e1;
    if (w < 2) {
      const rsrc_t rq = mk(p.wq, M * H * D * 2);
      const int mb = 160 * j + 80 * w + 20 * g;
      u32 xv[10];
#pragma unroll
      for (int f = 0; f < 10; ++f) xv[f] = __builtin_amdgcn_raw_buffer_load_b32(rx, (bsel * M + mb) * 2 + 4 * f, 0, 0);
      const u32 ep = __builtin_amdgcn_raw_buffer_load_b32(rw, W_EPOCH, 0, SC1);
      v4u wa[20];
      const int vo = (mb * (H * D) + h * D + 8 * r) * 2;
#pragma unroll
      for (int f = 0; f < 20; ++f) wa[f] = ldw(rq, vo, f * H * D * 2);
      SB();
      __builtin_amdgcn_s_barrier();
      SB();
      STAMP(1);
      v4u sel[20];
#pragma unroll
      for (int f = 0; f < 10; ++f) { sel[2 * f] = selfrag(duplo(xv[f]), mask); sel[2 * f + 1] = selfrag(duphi(xv[f]), mask); }
#pragma unroll
      for (int f = 0; f < 20; ++f) PIN(sel[f]);
      e1 = __builtin_amdgcn_readfirstlane(ep) + 1u;
      int o = W_QP + (ac * 2 + w) * 1024 + (g >> 1) * 512 + (r >> 2) * 128 + (g & 1) * 64 + (r & 3) * 16;
      PIN(o);
      SB();
      STAMP(2);
      f4 acc = {0.f, 0.f, 0.f, 0.f};
#pragma unroll
      for (int f = 0; f < 20; ++f) acc = mfma16(sel[f], wa[f], acc);
      sth(v4u{pk2(acc.x, acc.y), e1, pk2(acc.z, acc.w), e1}, rw, o);
      STAMP(3);
    } else if (w < 4) {
      const int w2 = w - 2;
      if (l < 16) sth(sent4, rw, W_QA + (((l >> 3) * H + h) * DC + 128 * j + 64 * w2) * 2 + (l & 7) * 16);
      const u32 ep = __builtin_amdgcn_raw_buffer_load_b32(rw, W_EPOCH, 0, SC1);
      const u16* src = p.wuk + ((size_t)(128 * j + 64 * w2 + g) * H + h) * D;
      LAS unsigned char* lk = L + w2 * 16384;
      __builtin_amdgcn_s_barrier();
      SB();
#pragma unroll
      for (int n = 0; n < 16; ++n) dma(src + n * 4 * H * D + 8 * (r ^ n), lk + n * 1024);
      DRAIN();
      STAMP(1);
      e1 = __builtin_amdgcn_readfirstlane(ep) + 1u;
      if (l == 0) sth2(v2u{e1, e1 * 0x9E3779B1u ^ 0x5bd1e995u}, rw, W_FLAG + (256 + ac * 2 + w2) * 128);
      v4u wk[16];
#pragma unroll
      for (int t = 0; t < 4; ++t)
#pragma unroll
        for (int s = 0; s < 4; ++s) wk[t * 4 + s] = *(const LAS v4u*)(lk + (4 * r + t) * 256 + 16 * ((4 * s + g) ^ r));
#pragma unroll
      for (int i = 0; i < 16; ++i) PIN(wk[i]);
      int oa = W_QA + (((g >> 1) * H + h) * DC + 128 * j + 64 * w2 + 4 * r + 2 * (g & 1)) * 2;
      PIN(oa);
      STAMP(2);
      v4u u[8];
      const int o = W_QP + ((h * 4 + ((r >> 1) & 3)) * 2 + (r & 1)) * 1024 + (r >> 3) * 512 + 16 * g;
#pragma unroll 1
      for (u32 sp = 0; sp < SPIN_CAP; ++sp) {
        CBAR();
#pragma unroll
        for (int n = 0; n < 8; ++n) u[n] = ldh(rw, o + 64 * n);
        u32 mn = e1, mx = e1;
#pragma unroll
        for (int n = 0; n < 8; ++n) { mn = umin(mn, umin(u[n].y, u[n].w)); mx = umax(mx, umax(u[n].y, u[n].w)); }
        if (!__any(mn != mx)) break;
      }
      STAMP(4);
      f4 a[4];
#pragma unroll
      for (int t = 0; t < 4; ++t) a[t] = f4{0.f, 0.f, 0.f, 0.f};
#pragma unroll
      for (int s = 0; s < 4; ++s) {
        const v4u qf = {u[2 * s].x, u[2 * s].z, u[2 * s + 1].x, u[2 * s + 1].z};
#pragma unroll
        for (int t = 0; t < 4; ++t) a[t] = mfma16(qf, wk[t * 4 + s], a[t]);
      }
      float st[4];
#pragma unroll
      for (int t = 0; t < 4; ++t) st[t] = (a[t].x + a[t].y) + (a[t].z + a[t].w);
      sth1(guard(pk2(swap16add(st[0], st[2]), swap16add(st[1], st[3]))), rw, oa);
      STAMP(5);
    } else {
      const u32 ep = __builtin_amdgcn_raw_buffer_load_b32(rw, W_EPOCH, 0, SC1);
      if (w == 4) {
#pragma unroll
        for (int i = 0; i < 5; ++i) sth(sent4, rw, W_PO + ac * 5120 + i * 1024 + l * 16);
      }
      __builtin_amdgcn_s_barrier();
      DRAIN();
      e1 = __builtin_amdgcn_readfirstlane(ep) + 1u;
      if (w == 4 && l == 0) sth2(v2u{e1, e1 * 0x9E3779B1u ^ 0x5bd1e995u}, rw, W_FLAG + ac * 128);
      asm volatile("" :: "v"(cpf));
      STAMP(1);
    }
    const v2u tag = {e1, e1 * 0x9E3779B1u ^ 0x5bd1e995u};
    SB();
    __builtin_amdgcn_s_barrier();
    SB();
    const int cj = j >> 1, dj = j & 1;
    {
      const u16* sv = p.wuv + ((size_t)(256 * cj + 32 * w + (l >> 3)) * H + h) * D + 64 * dj;
#pragma unroll
      for (int n = 0; n < 4; ++n) {
        const int fz = 2 * (((l >> 4) & 1) | ((n & 1) << 1));
        dma(sv + n * 8 * H * D + 8 * ((l & 7) ^ fz), L + (w * 4 + n) * 1024);
      }
      const u16* so = p.wo + ((size_t)h * D + 64 * dj + 8 * w + g) * M;
      const int fo = 2 * (g | ((w & 1) << 2));
#pragma unroll
      for (int n = 0; n < 10; ++n) dma(so + (n & 1) * 4 * M + (n >> 1) * 128 + 8 * (r ^ fo), L + 32768 + (n >> 1) * 16384 + (8 * w + 4 * (n & 1)) * 256);
    }
    const int cb_b = w >> 2, cb = (w >> 1) & 1, kh = w & 1;
    DRAIN();
    __builtin_amdgcn_s_barrier();
    CBAR();
    STAMP(6);
    v4u wv[8], wo[12];
    {
      const int q = r >> 2, pp = l & 3;
#pragma unroll
      for (int ks = 0; ks < 8; ++ks) {
        const int row = 32 * ks + 8 * g + q, fz = 2 * (((q >> 1) & 1) | ((g & 1) << 1));
        LAS unsigned char* a = L + row * 128 + 16 * ((2 * (w & 3) + (pp >> 1)) ^ fz) + 8 * (pp & 1);
        wv[ks] = trfrag(a, a + 512);
      }
#pragma unroll
      for (int pi = 0; pi < 3; ++pi) {
        const int P = (pi == 2 ? (w & 3) : w) + 8 * pi;
#pragma unroll
        for (int tt = 0; tt < 2; ++tt)
#pragma unroll
          for (int ks = 0; ks < 2; ++ks) {
            const int row = 32 * ks + 8 * g + q, fo = 2 * (q | ((g & 1) << 2));
            LAS unsigned char* a = L + 32768 + (P >> 2) * 16384 + row * 256 + 16 * ((4 * (P & 3) + 2 * tt + (pp >> 1)) ^ fo) + 8 * (pp & 1);
            wo[pi * 4 + tt * 2 + ks] = trfrag(a, a + 1024);
          }
      }
#pragma unroll
      for (int i = 0; i < 8; ++i) PIN(wv[i]);
#pragma unroll
      for (int i = 0; i < 12; ++i) PIN(wo[i]);
    }
    wait_flags(rw, 128 + cb_b * 32 + (l & 31), tag);
    v4u wsel[4];
    {
      v2u ml;
#pragma unroll 1
      for (u32 sp = 0; sp < SPIN_CAP; ++sp) {
        CBAR();
        ml = ldh2(rw, W_ML + ((cb_b * 32 + (l & 31)) * H + h) * 8);
        if (!__any(umax(ml.x, ml.y) == SENT)) break;
      }
      const float mm = __uint_as_float(ml.x), ll = __uint_as_float(ml.y);
      float mx = mm;
#pragma unroll
      for (int o = 1; o < 32; o <<= 1) mx = fmaxf(mx, __shfl_xor(mx, o));
      const float e = __builtin_amdgcn_exp2f(mm - mx);
      float sum = e * ll;
#pragma unroll
      for (int o = 1; o < 32; o <<= 1) sum += __shfl_xor(sum, o);
      const float wgt = e * __builtin_amdgcn_rcpf(sum);
      const u32 wb = pk2(wgt, wgt);
#pragma unroll
      for (int i = 0; i < 4; ++i) { wsel[i] = selfrag(__shfl(wb, 16 * kh + 4 * i + g), mask); PIN(wsel[i]); }
    }
    constexpr int L_A2 = 114688, L_A3 = L_A2 + 4 * 528;
    int a2w = L_A2 + (cb_b * 2 + kh) * 528 + (128 * cb + 8 * r + 4 * (g & 1)) * 2;
    int a2r = L_A2 + (r & 3) * 528 + 16 * g;
    int a3w = L_A3 + (g & 1) * 144 + (16 * (w & 3) + r) * 2;
    int a3r = L_A3 + (r >> 3) * 144 + 16 * g;
    int po = W_PO + ac * 5120 + ((g >> 1) * M + 32 * w + 16 * (g & 1) + r) * 4;
    PIN(a2w); PIN(a2r); PIN(a3w); PIN(a3r); PIN(po);
    STAMP(7);
    v4u of[4];
    {
      const int o = W_OP + (((cb_b * 32 + 16 * kh + g) * H + h) * DC + 256 * cj + 128 * cb + 8 * r) * 2;
#pragma unroll 1
      for (u32 sp = 0; sp < SPIN_CAP; ++sp) {
        CBAR();
#pragma unroll
        for (int i = 0; i < 4; ++i) of[i] = ldhs(rw, o, i * 4 * H * DC * 2);
        if (!__any(umax(umax(max4(of[0]), max4(of[1])), umax(max4(of[2]), max4(of[3]))) == SENT)) break;
      }
    }
    STAMP(8);
    {
      f4 acc = {0.f, 0.f, 0.f, 0.f};
#pragma unroll
      for (int i = 0; i < 4; ++i) acc = mfma16(wsel[i], of[i], acc);
      *(LAS v2u*)(L + a2w) = v2u{pk2(acc.x, acc.y), pk2(acc.z, acc.w)};
    }
    LBAR();
    if (w < 4) {
      v4u af[8];
#pragma unroll
      for (int ks = 0; ks < 8; ++ks) af[ks] = *(const LAS v4u*)(L + a2r + 64 * ks);
      SB();
      f4 acc = {0.f, 0.f, 0.f, 0.f};
#pragma unroll
      for (int ks = 0; ks < 8; ++ks) acc = mfma16(af[ks], wv[ks], acc);
      const float v = (g & 1) ? acc.z + acc.w : acc.x + acc.y;
      *(LAS u16*)(L + a3w) = (u16)pk2(v, v);
    }
    LBAR();
    {
      const v4u a0 = *(const LAS v4u*)(L + a3r), a1 = *(const LAS v4u*)(L + a3r + 64);
#pragma unroll
      for (int pi = 0; pi < 3; ++pi) {
        if (pi == 2 && w >= 4) break;
        f4 t0 = {0.f, 0.f, 0.f, 0.f}, t1 = {0.f, 0.f, 0.f, 0.f};
        t0 = mfma16(a0, wo[pi * 4 + 0], t0); t1 = mfma16(a0, wo[pi * 4 + 2], t1);
        t0 = mfma16(a1, wo[pi * 4 + 1], t0); t1 = mfma16(a1, wo[pi * 4 + 3], t1);
        sth1(guard(__float_as_uint((g & 1) ? t1.x : t0.x)), rw, po + pi * 1024);
      }
    }
    STAMP(9);
  } else if (bid < 192) {
    const int i2 = bid - 128;
    const int b = (i2 & 7) >> 2, ks = (i2 & 3) + 4 * (i2 >> 3), bk = b * 32 + ks;
    if (B_DELAY) __builtin_amdgcn_s_sleep(B_DELAY);
    {
      const u16* src = p.ckv + ((size_t)b * SK + 128 * ks) * DC;
#pragma unroll
      for (int n = 0; n < 16; ++n) {
        const int r0 = (w * 4 + (n >> 2)) * 4, blk = n & 3, row = r0 + g;
        dma(src + row * DC + 128 * blk + 8 * (r ^ swz(row)), L + blk * 32768 + r0 * 256);
      }
    }
    SB();
#pragma unroll
    for (int i = 0; i < 4; ++i) sth(sent4, rw, W_OP + bk * 32768 + (i * 512 + tid) * 16);
    if (tid < 16) sth(sent4, rw, W_ML + bk * 256 + tid * 16);
    const u32 ep = __builtin_amdgcn_raw_buffer_load_b32(rw, W_EPOCH, 0, SC1);
    STAMP(1);
    DRAIN();
    const u32 e1 = __builtin_amdgcn_readfirstlane(ep) + 1u;
    const v2u tag = {e1, e1 * 0x9E3779B1u ^ 0x5bd1e995u};
    asm volatile("" :: "v"(cpf));
    __builtin_amdgcn_s_barrier();
    CBAR();
    if (tid == 0) sth2(tag, rw, W_FLAG + (128 + bk) * 128);
    STAMP(2);
    v4u fr[32];
    const int hh = l >> 5;
    if (w < 4) {
      const int row = 32 * w + (l & 31), sw = swz(row);
#pragma unroll
      for (int s = 0; s < 32; ++s) fr[s] = *(const LAS v4u*)(L + (s >> 3) * 32768 + row * 256 + 16 * ((((2 * s) & 15) + hh) ^ sw));
    } else {
      const int bk2 = (l >> 4) & 1, q = (l & 15) >> 2, pp = l & 3;
#pragma unroll
      for (int ct = 0; ct < 4; ++ct)
#pragma unroll
        for (int kk = 0; kk < 8; ++kk) {
          const int r0 = 16 * kk + 4 * hh + q, r1 = r0 + 8, ch = 4 * ct + 2 * bk2 + (pp >> 1);
          LAS unsigned char* base = L + (w - 4) * 32768 + 8 * (pp & 1);
          fr[ct * 8 + kk] = trfrag(base + r0 * 256 + 16 * (ch ^ swz(r0)), base + r1 * 256 + 16 * (ch ^ swz(r1)));
        }
    }
    wait_flags(rw, 256 + (w + 8 * ((l & 31) >> 3)) * 8 + (l & 7), tag);
#pragma unroll
    for (int s = 0; s < 32; ++s) PIN(fr[s]);
    LBAR();
    STAMP(3);
    constexpr int L_MX = 33280, L_LS = 34304, L_P = 35328, L_SL = 43520;
    {
      v4u qv[4];
      const int o = W_QA + b * 32768 + tid * 16;
#pragma unroll 1
      for (u32 sp = 0; sp < SPIN_CAP; ++sp) {
        CBAR();
#pragma unroll
        for (int i = 0; i < 4; ++i) qv[i] = ldhs(rw, o, i * 8192);
        if (!__any(umax(umax(max4(qv[0]), max4(qv[1])), umax(max4(qv[2]), max4(qv[3]))) == SENT)) break;
      }
      STAMP(4);
#pragma unroll
      for (int i = 0; i < 4; ++i) *(LAS v4u*)(L + (w + 8 * i) * 1040 + l * 16) = qv[i];
    }
    LBAR();
    STAMP(5);
    if (w < 4) {
      f16v sacc;
#pragma unroll
      for (int i = 0; i < 16; ++i) sacc[i] = 0.f;
      const LAS unsigned char* qb = L + (l & 31) * 1040 + 16 * hh;
      v4u qf[12];
#pragma unroll
      for (int s = 0; s < 12; ++s) qf[s] = *(const LAS v4u*)(qb + 32 * s);
      SB();
#pragma unroll
      for (int s = 0; s < 32; ++s) {
        sacc = mfma32(fr[s], qf[s % 12], sacc);
        if (s + 12 < 32) qf[s % 12] = *(const LAS v4u*)(qb + 32 * (s + 12));
        SB();
      }
      float mx = sacc[0];
#pragma unroll
      for (int i = 1; i < 16; ++i) mx = fmaxf(mx, sacc[i]);
      *(LAS float*)(L + L_MX + ((l & 31) * 8 + w * 2 + hh) * 4) = mx;
      LBAR();
      STAMP(6);
      const f4 m0 = *(const LAS f4*)(L + L_MX + (l & 31) * 32), m1 = *(const LAS f4*)(L + L_MX + (l & 31) * 32 + 16);
      mx = fmaxf(fmaxf(fmaxf(m0.x, m0.y), fmaxf(m0.z, m0.w)), fmaxf(fmaxf(m1.x, m1.y), fmaxf(m1.z, m1.w))) * SCL;
      float e[16];
#pragma unroll
      for (int i = 0; i < 16; ++i) e[i] = __builtin_amdgcn_exp2f(__builtin_fmaf(sacc[i], SCL, -mx));
      *(LAS v4u*)(L + L_P + ((2 * w) * 64 + l) * 16) = v4u{pk2(e[0], e[1]), pk2(e[2], e[3]), pk2(e[4], e[5]), pk2(e[6], e[7])};
      *(LAS v4u*)(L + L_P + ((2 * w + 1) * 64 + l) * 16) = v4u{pk2(e[8], e[9]), pk2(e[10], e[11]), pk2(e[12], e[13]), pk2(e[14], e[15])};
      LBAR();
      STAMP(7);
      float sum = 0.f;
#pragma unroll
      for (int i = 0; i < 16; ++i) sum += e[i];
      *(LAS float*)(L + L_LS + ((l & 31) * 8 + w * 2 + hh) * 4) = sum;
      LBAR();
      if (w == 0 && l < 32) {
        const f4 s0 = *(const LAS f4*)(L + L_LS + l * 32), s1 = *(const LAS f4*)(L + L_LS + l * 32 + 16);
        sum = ((s0.x + s0.y) + (s0.z + s0.w)) + ((s1.x + s1.y) + (s1.z + s1.w));
        sth2(v2u{guard(__float_as_uint(mx)), guard(__float_as_uint(sum))}, rw, W_ML + bk * 256 + l * 8);
      }
    } else {
      LAS unsigned char* slab = L + L_SL + (w - 4) * 8704;
      int sw_ = (l & 31) * 272 + 8 * hh, sr_ = (l >> 3) * 272 + 16 * (l & 7);
      int oo = W_OP + bk * 32768 + (l >> 3) * 1024 + (128 * (w - 4) + 8 * (l & 7)) * 2;
      PIN(sw_); PIN(sr_); PIN(oo);
      __builtin_amdgcn_s_barrier();
      __builtin_amdgcn_s_barrier();
      CBAR();
      STAMP(7);
      v4u pf[8];
#pragma unroll
      for (int kk = 0; kk < 8; ++kk) pf[kk] = *(const LAS v4u*)(L + L_P + (kk * 64 + l) * 16);
      SB();
      f16v o[4];
#pragma unroll
      for (int ct = 0; ct < 4; ++ct) {
#pragma unroll
        for (int i = 0; i < 16; ++i) o[ct][i] = 0.f;
#pragma unroll
        for (int kk = 0; kk < 8; ++kk) o[ct] = mfma32(fr[ct * 8 + kk], pf[kk], o[ct]);
        if (ct == 0) __builtin_amdgcn_s_barrier();
#pragma unroll
        for (int g4 = 0; g4 < 4; ++g4)
          *(LAS v2u*)(slab + sw_ + (32 * ct + 8 * g4) * 2) = v2u{pk2(o[ct][4 * g4], o[ct][4 * g4 + 1]), pk2(o[ct][4 * g4 + 2], o[ct][4 * g4 + 3])};
        if (ct & 1) {
#pragma unroll
          for (int n = 0; n < 4; ++n) {
            const v4u v = *(const LAS v4u*)(slab + sr_ + n * 8 * 272 + 128 * (ct >> 1));
            __builtin_amdgcn_raw_buffer_store_b128(guard4(v), rw, oo + 128 * (ct >> 1), n * 8192, SC1);
          }
        }
      }
    }
    STAMP(8);
  } else {
    const int i3 = bid - 192;
    if (i3 >= 54) { asm volatile("" :: "v"(cpf)); return; }
    const u32 ep = __builtin_amdgcn_raw_buffer_load_b32(rw, W_EPOCH, 0, SC1);
    const u32 e1 = __builtin_amdgcn_readfirstlane(ep) + 1u;
    asm volatile("" :: "v"(cpf));
    const v2u tag = {e1, e1 * 0x9E3779B1u ^ 0x5bd1e995u};
    if (F_DELAY) __builtin_amdgcn_s_sleep(F_DELAY);
    if (i3 < 27) {
      STAMP(1);
      u32 xv[10];
#pragma unroll
      for (int f = 0; f < 10; ++f) xv[f] = __builtin_amdgcn_raw_buffer_load_b32(rx, (bsel * M + 80 * w + 20 * g) * 2 + 4 * f, 0, 0);
      v4u wf[20];
      const GAS u16* src = (const GAS u16*)((r >> 3) ? p.wu : p.wg) + (80 * w + 20 * g) * F + 64 * i3 + 8 * (r & 7);
#pragma unroll
      for (int f = 0; f < 20; ++f) wf[f] = __builtin_nontemporal_load((const GAS v4u*)(src + f * F));
      SB();
      f4 acc = {0.f, 0.f, 0.f, 0.f};
#pragma unroll
      for (int f = 0; f < 10; ++f) {
        acc = mfma16(selfrag(duplo(xv[f]), mask), wf[2 * f], acc);
        acc = mfma16(selfrag(duphi(xv[f]), mask), wf[2 * f + 1], acc);
      }
      *(LAS f4*)(L + (w * 64 + l) * 16) = acc;
      STAMP(2);
      LBAR();
      if (w == 0) {
        f4 s = *(const LAS f4*)(L + l * 16);
#pragma unroll
        for (int i = 1; i < 8; ++i) s += *(const LAS f4*)(L + (i * 64 + l) * 16);
        float hv[4];
#pragma unroll
        for (int i = 0; i < 4; ++i) { const float gs = s[i], us = ror8(s[i]); hv[i] = gs / (1.f + __expf(-gs)) * us; }
        if (r < 8) sth(v4u{pk2(hv[0], hv[1]), e1, pk2(hv[2], hv[3]), e1}, rw, W_H1 + i3 * 512 + (g >> 1) * 256 + (2 * r + (g & 1)) * 16);
      }
      STAMP(3);
    } else {
      const int u = i3 - 27;
      if (tid < 320) sth(sent4, rw, W_PF + u * 5120 + tid * 16);
      DRAIN();
      __builtin_amdgcn_s_barrier();
      if (tid == 0) sth2(tag, rw, W_FLAG + (192 + u) * 128);
      STAMP(1);
      if (w < 5) {
        const rsrc_t rd = mk(p.wd, F * M * 2);
        v4u wdn[16];
        const int vo = ((64 * u + 16 * g) * M + 128 * w + 8 * r) * 2;
#pragma unroll
        for (int f = 0; f < 16; ++f) wdn[f] = ldw(rd, vo, f * M * 2);
        SB();
        v4u z[4];
#pragma unroll 1
        for (u32 sp = 0; sp < SPIN_CAP; ++sp) {
          CBAR();
#pragma unroll
          for (int n = 0; n < 4; ++n) z[n] = ldh(rw, W_H1 + u * 512 + bsel * 256 + g * 64 + n * 16);
          u32 mn = e1, mx = e1;
#pragma unroll
          for (int n = 0; n < 4; ++n) { mn = umin(mn, umin(z[n].y, z[n].w)); mx = umax(mx, umax(z[n].y, z[n].w)); }
          if (!__any(mn != mx)) break;
          __builtin_amdgcn_s_sleep(2);
        }
        STAMP(2);
        f4 acc = {0.f, 0.f, 0.f, 0.f};
#pragma unroll
        for (int n = 0; n < 4; ++n) {
          acc = mfma16(selfrag(duplo(z[n].x), mask), wdn[4 * n], acc);
          acc = mfma16(selfrag(duphi(z[n].x), mask), wdn[4 * n + 1], acc);
          acc = mfma16(selfrag(duplo(z[n].z), mask), wdn[4 * n + 2], acc);
          acc = mfma16(selfrag(duphi(z[n].z), mask), wdn[4 * n + 3], acc);
        }
        sth(guard4(__builtin_bit_cast(v4u, acc)), rw, W_PF + u * 5120 + ((g >> 1) * M + 128 * w + 8 * r + 4 * (g & 1)) * 4);
      }
      STAMP(3);
    }
    if (i3 < 40) {
      const int du = i3, b = du / 20;
      float gate;
      {
        const rsrc_t rg = mk(p.gw, M * 2);
        float s = 0.f;
#pragma unroll
        for (int i = 0; i < 5; ++i) {
          const u32 xa = __builtin_amdgcn_raw_buffer_load_b32(rx, (b * M + 128 * i + 2 * l) * 2, 0, 0);
          const u32 ga = __builtin_amdgcn_raw_buffer_load_b32(rg, (128 * i + 2 * l) * 2, 0, 0);
          s += bflo(xa) * bflo(ga) + bfhi(xa) * bfhi(ga);
        }
#pragma unroll
        for (int o = 1; o < 64; o <<= 1) s += __shfl_xor(s, o);
        gate = 1.f / (1.f + __expf(-s));
      }
      const int pr = tid >> 3;
      wait_flags(rw, (l & 16) ? 192 + (int)umin(8 * w + (l & 7), 26) : 8 * w + (l & 7) + 8 * (l & 8), tag);
      f4 v = {0.f, 0.f, 0.f, 0.f};
      if (w < 4) {
        v4u t;
#pragma unroll 1
        for (u32 sp = 0; sp < SPIN_CAP; ++sp) {
          CBAR();
          t = ldh(rw, W_PF + (int)umin(pr, 26) * 5120 + du * 128 + (l & 7) * 16);
          if (!__any(max4(t) == SENT)) break;
          __builtin_amdgcn_s_sleep(2);
        }
        if (pr < 27) v = __builtin_bit_cast(f4, t) * (1.f - gate);
      }
      int lw = ((4 * (l & 7) + g) * 8 + w) * 4;
      PIN(lw); PIN(v);
      LAS unsigned char* LD = L + 16384;
      STAMP(9);
      {
        v4u t0, t1;
        const int o = W_PO + pr * 5120 + du * 128 + (l & 7) * 16;
#pragma unroll 1
        for (u32 sp = 0; sp < SPIN_CAP; ++sp) {
          CBAR();
          t0 = ldh(rw, o); t1 = ldhs(rw, o, 64 * 5120);
          if (!__any(umax(max4(t0), max4(t1)) == SENT)) break;
        }
        v += (__builtin_bit_cast(f4, t0) + __builtin_bit_cast(f4, t1)) * gate;
      }
      STAMP(10);
      float s = swap32add(swap16add(v.x, v.y), swap16add(v.z, v.w));
      s += ror8(s);
      *(LAS float*)(LD + lw) = s;
      LBAR();
      if (w == 0) {
        const f4 s0 = *(const LAS f4*)(LD + (l & 31) * 32), s1 = *(const LAS f4*)(LD + (l & 31) * 32 + 16);
        const float t = (((s0.x + s0.y) + (s0.z + s0.w)) + ((s1.x + s1.y) + (s1.z + s1.w)));
        if (l < 32) p.out[32 * du + l] = (u16)pk2(t, t);
        if (du == 0 && l == 0) sth1(e1, rw, W_EPOCH);
      }
      STAMP(11);
    }
  }
}

extern "C" void kernel_launch(void* const* d_in, const int* in_sizes, int n_in,
                              void* d_out, int out_size, void* d_ws, size_t ws_size,
                              hipStream_t stream) {
  Params p;
  p.x = (const u16*)d_in[0]; p.ckv = (const u16*)d_in[1]; p.wq = (const u16*)d_in[2]; p.wuk = (const u16*)d_in[3];
  p.wuv = (const u16*)d_in[4]; p.wo = (const u16*)d_in[5]; p.wg = (const u16*)d_in[7]; p.wu = (const u16*)d_in[8];
  p.wd = (const u16*)d_in[9]; p.gw = (const u16*)d_in[10]; p.out = (u16*)d_out; p.ws = (unsigned char*)d_ws;
  (void)hipFuncSetAttribute((const void*)mk_kernel, hipFuncAttributeMaxDynamicSharedMemorySize, LDS_BYTES);
  mk_kernel<<<256, NT, LDS_BYTES, stream>>>(p);
}
```

```cpp
#define DIAG 3
#include <hip/hip_runtime.h>
#include <stdint.h>

#define STAMPS 0
#define F_DELAY 30
#define B_DELAY 0

typedef unsigned short u16;
typedef unsigned int u32;
typedef unsigned long long u64;
typedef u32 v4u __attribute__((ext_vector_type(4)));
typedef u32 v2u __attribute__((ext_vector_type(2)));
typedef float f4 __attribute__((ext_vector_type(4)));
typedef float f2 __attribute__((ext_vector_type(2)));
typedef float f16v __attribute__((ext_vector_type(16)));
typedef short bf8 __attribute__((ext_vector_type(8)));
typedef short s4 __attribute__((ext_vector_type(4)));
typedef __bf16 bf2 __attribute__((ext_vector_type(2)));
#define GAS __attribute__((address_space(1)))
#define LAS __attribute__((address_space(3)))
#define DI __device__ __forceinline__
typedef __amdgpu_buffer_rsrc_t rsrc_t;

constexpr int NB = 2, H = 32, SK = 4096, D = 128, M = 640, DC = 512, F = 1728;
constexpr int NT = 512;
constexpr int W_EPOCH = 0;
constexpr int W_H1 = 4096;
constexpr int W_QA = 65536;
constexpr int W_ML = 131072;
constexpr int W_FLAG = 196608;
constexpr int W_QP = 262144;
constexpr int W_PO = 524288;
constexpr int W_PF = 1179648;
constexpr int W_OP = 2097152;
constexpr int W_END = 4194304;
constexpr int W_DBG = 8388608;
constexpr int LDS_BYTES = 131072 + 1024;
constexpr int SC1 = 16, NTL = 2;
constexpr u32 SENT = 0xFFFFFFFFu;
constexpr u32 SPIN_CAP = 1u << 20;
constexpr int CODE_PF_BYTES = 16384;
constexpr float SCL = 0.08838834764831845f * 1.4426950408889634f;

struct Params {
  const u16* x; const u16* ckv; const u16* wq; const u16* wuk; const u16* wuv; const u16* wo;
  const u16* wg; const u16* wu; const u16* wd; const u16* gw; u16* out; unsigned char* ws;
};

DI rsrc_t mk(const void* p, int bytes) { return __builtin_amdgcn_make_buffer_rsrc((void*)p, 0, bytes, 0x00020000); }
DI v4u ldw(rsrc_t r, int voff, int soff) { return __builtin_amdgcn_raw_buffer_load_b128(r, voff, soff, NTL); }
DI v4u ldh(rsrc_t r, int voff) { return __builtin_amdgcn_raw_buffer_load_b128(r, voff, 0, SC1); }
DI v4u ldhs(rsrc_t r, int voff, int soff) { return __builtin_amdgcn_raw_buffer_load_b128(r, voff, soff, SC1); }
DI v2u ldh2(rsrc_t r, int voff) { return __builtin_amdgcn_raw_buffer_load_b64(r, voff, 0, SC1); }
DI void sth(v4u v, rsrc_t r, int voff) { __builtin_amdgcn_raw_buffer_store_b128(v, r, voff, 0, SC1); }
DI void sth2(v2u v, rsrc_t r, int voff) { __builtin_amdgcn_raw_buffer_store_b64(v, r, voff, 0, SC1); }
DI void sth1(u32 v, rsrc_t r, int voff) { __builtin_amdgcn_raw_buffer_store_b32(v, r, voff, 0, SC1); }
DI u32 umin(u32 a, u32 b) { return a < b ? a : b; }
DI u32 umax(u32 a, u32 b) { return a > b ? a : b; }
DI u32 guard(u32 v) { return umin(v, 0xFFFFFFFEu); }
DI v4u guard4(v4u v) { return v4u{guard(v.x), guard(v.y), guard(v.z), guard(v.w)}; }
DI u32 max4(v4u v) { return umax(umax(v.x, v.y), umax(v.z, v.w)); }
DI u32 pk2(float lo, float hi) { bf2 r = {(__bf16)lo, (__bf16)hi}; return __builtin_bit_cast(u32, r); }
DI float bflo(u32 v) { return __uint_as_float(v << 16); }
DI float bfhi(u32 v) { return __uint_as_float(v & 0xFFFF0000u); }
DI f4 mfma16(v4u a, v4u b, f4 c) { return __builtin_amdgcn_mfma_f32_16x16x32_bf16(__builtin_bit_cast(bf8, a), __builtin_bit_cast(bf8, b), c, 0, 0, 0); }
DI f16v mfma32(v4u a, v4u b, f16v c) { return __builtin_amdgcn_mfma_f32_32x32x16_bf16(__builtin_bit_cast(bf8, a), __builtin_bit_cast(bf8, b), c, 0, 0, 0); }
#define CBAR() asm volatile("" ::: "memory")
#define SB() __builtin_amdgcn_sched_barrier(0)
#define PIN(x) asm volatile("" : "+v"(x))
#define DRAIN() asm volatile("s_waitcnt vmcnt(0)" ::: "memory")
#define LBAR() do { asm volatile("s_waitcnt lgkmcnt(0)" ::: "memory"); __builtin_amdgcn_s_barrier(); CBAR(); } while (0)
DI float ror8(float v) { return __builtin_bit_cast(float, __builtin_amdgcn_update_dpp(0, __builtin_bit_cast(int, v), 0x128, 0xf, 0xf, false)); }
DI float swap16add(float x, float y) { auto t = __builtin_amdgcn_permlane16_swap(__float_as_uint(x), __float_as_uint(y), false, false); return __uint_as_float(t[0]) + __uint_as_float(t[1]); }
DI float swap32add(float x, float y) { auto t = __builtin_amdgcn_permlane32_swap(__float_as_uint(x), __float_as_uint(y), false, false); return __uint_as_float(t[0]) + __uint_as_float(t[1]); }
DI v4u selfrag(u32 vv, v4u mask) { return v4u{vv & mask.x, vv & mask.y, vv & mask.z, vv & mask.w}; }
DI u32 duplo(u32 v) { return __builtin_amdgcn_perm(v, v, 0x01000100u); }
DI u32 duphi(u32 v) { return __builtin_amdgcn_perm(v, v, 0x03020302u); }
DI int swz(int row) { return ((row & 3) << 2) | ((row >> 2) & 3); }
DI void dma(const u16* src, LAS unsigned char* dst) { __builtin_amdgcn_global_load_lds((const GAS u32*)src, (LAS u32*)dst, 16, 0, NTL); }
DI v4u trfrag(LAS unsigned char* a0, LAS unsigned char* a1) {
  const v2u lo = __builtin_bit_cast(v2u, __builtin_amdgcn_ds_read_tr16_b64_v4i16((LAS s4*)a0));
  const v2u hi = __builtin_bit_cast(v2u, __builtin_amdgcn_ds_read_tr16_b64_v4i16((LAS s4*)a1));
  return v4u{lo.x, lo.y, hi.x, hi.y};
}

#define ROLE_OF(b) ((b) < 128 ? 0 : (b) < 192 ? 1 : ((b) < 246 ? 2 : -1))
#define STAMP(k) do {} while (0)

DI void wait_flags(rsrc_t rw, int idx, v2u tag) {
#pragma unroll 1
  for (u32 sp = 0; sp < SPIN_CAP; ++sp) {
    CBAR();
    v2u f = ldh2(rw, W_FLAG + idx * 128);
    if (!__any((f.x != tag.x) | (f.y != tag.y))) break;
    __builtin_amdgcn_s_sleep(1);
  }
  CBAR();
}

__global__ void __launch_bounds__(NT, 2) mk_kernel(Params p) {
  extern __shared__ __attribute__((aligned(16))) unsigned char smem[];
  LAS unsigned char* L = (LAS unsigned char*)smem;
  const int tid = threadIdx.x, l = tid & 63, w = __builtin_amdgcn_readfirstlane(tid >> 6);
  const int bid = blockIdx.x;
  const int r = l & 15, g = l >> 4;
  STAMP(0);
  { const u64 t0 = __builtin_amdgcn_s_memrealtime(); if (tid == 0) __hip_atomic_store((GAS u64*)(p.ws + W_DBG) + bid, t0, __ATOMIC_RELAXED, __HIP_MEMORY_SCOPE_AGENT); }
  u64 pc0; asm volatile("s_getpc_b64 %0" : "=s"(pc0));
  u32 cpf = 0;
  if (w >= 6) cpf = ((const GAS u32*)(pc0 & ~127ull))[umin(tid - 384, CODE_PF_BYTES / 128 - 1) * 32];
  CBAR(); SB();
  const rsrc_t rw = mk(p.ws, W_END);
  const rsrc_t rx = mk(p.x, NB * M * 2);
  const v4u sent4 = {SENT, SENT, SENT, SENT};
  const u32 m16 = (l & 1) ? 0xFFFF0000u : 0x0000FFFFu;
  const int jq = (l & 7) >> 1;
  const v4u mask = {jq == 0 ? m16 : 0u, jq == 1 ? m16 : 0u, jq == 2 ? m16 : 0u, jq == 3 ? m16 : 0u};
  const int bsel = (l >> 3) & 1;

  if (__builtin_expect(bid < 128, 1)) {
    const int h = (bid & 7) * 4 + (bid >> 5), j = (bid >> 3) & 3, ac = h * 4 + j;
    u32 e1;
    if (w < 2) {
      const rsrc_t rq = mk(p.wq, M * H * D * 2);
      const int mb = 160 * j + 80 * w + 20 * g;
      u32 xv[10];
#pragma unroll
      for (int f = 0; f < 10; ++f) xv[f] = __builtin_amdgcn_raw_buffer_load_b32(rx, (bsel * M + mb) * 2 + 4 * f, 0, 0);
      const u32 ep = __builtin_amdgcn_raw_buffer_load_b32(rw, W_EPOCH, 0, SC1);
      v4u wa[20];
      const int vo = (mb * (H * D) + h * D + 8 * r) * 2;
#pragma unroll
      for (int f = 0; f < 20; ++f) wa[f] = ldw(rq, vo, f * H * D * 2);
      SB();
      __builtin_amdgcn_s_barrier();
      SB();
      STAMP(1);
      v4u sel[20];
#pragma unroll
      for (int f = 0; f < 10; ++f) { sel[2 * f] = selfrag(duplo(xv[f]), mask); sel[2 * f + 1] = selfrag(duphi(xv[f]), mask); }
#pragma unroll
      for (int f = 0; f < 20; ++f) PIN(sel[f]);
      e1 = __builtin_amdgcn_readfirstlane(ep) + 1u;
      int o = W_QP + (ac * 2 + w) * 1024 + (g >> 1) * 512 + (r >> 2) * 128 + (g & 1) * 64 + (r & 3) * 16;
      PIN(o);
      SB();
      STAMP(2);
      f4 acc = {0.f, 0.f, 0.f, 0.f};
#pragma unroll
      for (int f = 0; f < 20; ++f) acc = mfma16(sel[f], wa[f], acc);
      sth(v4u{pk2(acc.x, acc.y), e1, pk2(acc.z, acc.w), e1}, rw, o);
      STAMP(3);
    } else if (w < 4) {
      const int w2 = w - 2;
      if (l < 16) sth(sent4, rw, W_QA + (((l >> 3) * H + h) * DC + 128 * j + 64 * w2) * 2 + (l & 7) * 16);
      const u32 ep = __builtin_amdgcn_raw_buffer_load_b32(rw, W_EPOCH, 0, SC1);
      const u16* src = p.wuk + ((size_t)(128 * j + 64 * w2 + g) * H + h) * D;
      LAS unsigned char* lk = L + w2 * 16384;
      __builtin_amdgcn_s_barrier();
      SB();
#pragma unroll
      for (int n = 0; n < 16; ++n) dma(src + n * 4 * H * D + 8 * (r ^ n), lk + n * 1024);
      DRAIN();
      STAMP(1);
      e1 = __builtin_amdgcn_readfirstlane(ep) + 1u;
      if (l == 0) sth2(v2u{e1, e1 * 0x9E3779B1u ^ 0x5bd1e995u}, rw, W_FLAG + (256 + ac * 2 + w2) * 128);
      v4u wk[16];
#pragma unroll
      for (int t = 0; t < 4; ++t)
#pragma unroll
        for (int s = 0; s < 4; ++s) wk[t * 4 + s] = *(const LAS v4u*)(lk + (4 * r + t) * 256 + 16 * ((4 * s + g) ^ r));
#pragma unroll
      for (int i = 0; i < 16; ++i) PIN(wk[i]);
      int oa = W_QA + (((g >> 1) * H + h) * DC + 128 * j + 64 * w2 + 4 * r + 2 * (g & 1)) * 2;
      PIN(oa);
      STAMP(2);
      v4u u[8];
      const int o = W_QP + ((h * 4 + ((r >> 1) & 3)) * 2 + (r & 1)) * 1024 + (r >> 3) * 512 + 16 * g;
#pragma unroll 1
      for (u32 sp = 0; sp < SPIN_CAP; ++sp) {
        CBAR();
#pragma unroll
        for (int n = 0; n < 8; ++n) u[n] = ldh(rw, o + 64 * n);
        u32 mn = e1, mx = e1;
#pragma unroll
        for (int n = 0; n < 8; ++n) { mn = umin(mn, umin(u[n].y, u[n].w)); mx = umax(mx, umax(u[n].y, u[n].w)); }
        if (!__any(mn != mx)) break;
      }
      STAMP(4);
      f4 a[4];
#pragma unroll
      for (int t = 0; t < 4; ++t) a[t] = f4{0.f, 0.f, 0.f, 0.f};
#pragma unroll
      for (int s = 0; s < 4; ++s) {
        const v4u qf = {u[2 * s].x, u[2 * s].z, u[2 * s + 1].x, u[2 * s + 1].z};
#pragma unroll
        for (int t = 0; t < 4; ++t) a[t] = mfma16(qf, wk[t * 4 + s], a[t]);
      }
      float st[4];
#pragma unroll
      for (int t = 0; t < 4; ++t) st[t] = (a[t].x + a[t].y) + (a[t].z + a[t].w);
      sth1(guard(pk2(swap16add(st[0], st[2]), swap16add(st[1], st[3]))), rw, oa);
      STAMP(5);
    } else {
      const u32 ep = __builtin_amdgcn_raw_buffer_load_b32(rw, W_EPOCH, 0, SC1);
      if (w == 4) {
#pragma unroll
        for (int i = 0; i < 5; ++i) sth(sent4, rw, W_PO + ac * 5120 + i * 1024 + l * 16);
      }
      __builtin_amdgcn_s_barrier();
      DRAIN();
      e1 = __builtin_amdgcn_readfirstlane(ep) + 1u;
      if (w == 4 && l == 0) sth2(v2u{e1, e1 * 0x9E3779B1u ^ 0x5bd1e995u}, rw, W_FLAG + ac * 128);
      asm volatile("" :: "v"(cpf));
      STAMP(1);
    }
    const v2u tag = {e1, e1 * 0x9E3779B1u ^ 0x5bd1e995u};
    SB();
    __builtin_amdgcn_s_barrier();
    SB();
    const int cj = j >> 1, dj = j & 1;
    {
      const u16* sv = p.wuv + ((size_t)(256 * cj + 32 * w + (l >> 3)) * H + h) * D + 64 * dj;
#pragma unroll
      for (int n = 0; n < 4; ++n) {
        const int fz = 2 * (((l >> 4) & 1) | ((n & 1) << 1));
        dma(sv + n * 8 * H * D + 8 * ((l & 7) ^ fz), L + (w * 4 + n) * 1024);
      }
      const u16* so = p.wo + ((size_t)h * D + 64 * dj + 8 * w + g) * M;
      const int fo = 2 * (g | ((w & 1) << 2));
#pragma unroll
      for (int n = 0; n < 10; ++n) dma(so + (n & 1) * 4 * M + (n >> 1) * 128 + 8 * (r ^ fo), L + 32768 + (n >> 1) * 16384 + (8 * w + 4 * (n & 1)) * 256);
    }
    const int cb_b = w >> 2, cb = (w >> 1) & 1, kh = w & 1;
    DRAIN();
    __builtin_amdgcn_s_barrier();
    CBAR();
    STAMP(6);
    v4u wv[8], wo[12];
    {
      const int q = r >> 2, pp = l & 3;
#pragma unroll
      for (int ks = 0; ks < 8; ++ks) {
        const int row = 32 * ks + 8 * g + q, fz = 2 * (((q >> 1) & 1) | ((g & 1) << 1));
        LAS unsigned char* a = L + row * 128 + 16 * ((2 * (w & 3) + (pp >> 1)) ^ fz) + 8 * (pp & 1);
        wv[ks] = trfrag(a, a + 512);
      }
#pragma unroll
      for (int pi = 0; pi < 3; ++pi) {
        const int P = (pi == 2 ? (w & 3) : w) + 8 * pi;
#pragma unroll
        for (int tt = 0; tt < 2; ++tt)
#pragma unroll
          for (int ks = 0; ks < 2; ++ks) {
            const int row = 32 * ks + 8 * g + q, fo = 2 * (q | ((g & 1) << 2));
            LAS unsigned char* a = L + 32768 + (P >> 2) * 16384 + row * 256 + 16 * ((4 * (P & 3) + 2 * tt + (pp >> 1)) ^ fo) + 8 * (pp & 1);
            wo[pi * 4 + tt * 2 + ks] = trfrag(a, a + 1024);
          }
      }
#pragma unroll
      for (int i = 0; i < 8; ++i) PIN(wv[i]);
#pragma unroll
      for (int i = 0; i < 12; ++i) PIN(wo[i]);
    }
    wait_flags(rw, 128 + cb_b * 32 + (l & 31), tag);
    v4u wsel[4];
    {
      v2u ml;
#pragma unroll 1
      for (u32 sp = 0; sp < SPIN_CAP; ++sp) {
        CBAR();
        ml = ldh2(rw, W_ML + ((cb_b * 32 + (l & 31)) * H + h) * 8);
        if (!__any(umax(ml.x, ml.y) == SENT)) break;
      }
      const float mm = __uint_as_float(ml.x), ll = __uint_as_float(ml.y);
      float mx = mm;
#pragma unroll
      for (int o = 1; o < 32; o <<= 1) mx = fmaxf(mx, __shfl_xor(mx, o));
      const float e = __builtin_amdgcn_exp2f(mm - mx);
      float sum = e * ll;
#pragma unroll
      for (int o = 1; o < 32; o <<= 1) sum += __shfl_xor(sum, o);
      const float wgt = e * __builtin_amdgcn_rcpf(sum);
      const u32 wb = pk2(wgt, wgt);
#pragma unroll
      for (int i = 0; i < 4; ++i) { wsel[i] = selfrag(__shfl(wb, 16 * kh + 4 * i + g), mask); PIN(wsel[i]); }
    }
    constexpr int L_A2 = 114688, L_A3 = L_A2 + 4 * 528;
    int a2w = L_A2 + (cb_b * 2 + kh) * 528 + (128 * cb + 8 * r + 4 * (g & 1)) * 2;
    int a2r = L_A2 + (r & 3) * 528 + 16 * g;
    int a3w = L_A3 + (g & 1) * 144 + (16 * (w & 3) + r) * 2;
    int a3r = L_A3 + (r >> 3) * 144 + 16 * g;
    int po = W_PO + ac * 5120 + ((g >> 1) * M + 32 * w + 16 * (g & 1) + r) * 4;
    PIN(a2w); PIN(a2r); PIN(a3w); PIN(a3r); PIN(po);
    STAMP(7);
    v4u of[4];
    {
      const int o = W_OP + (((cb_b * 32 + 16 * kh + g) * H + h) * DC + 256 * cj + 128 * cb + 8 * r) * 2;
#pragma unroll 1
      for (u32 sp = 0; sp < SPIN_CAP; ++sp) {
        CBAR();
#pragma unroll
        for (int i = 0; i < 4; ++i) of[i] = ldhs(rw, o, i * 4 * H * DC * 2);
        if (!__any(umax(umax(max4(of[0]), max4(of[1])), umax(max4(of[2]), max4(of[3]))) == SENT)) break;
      }
    }
    STAMP(8);
    {
      f4 acc = {0.f, 0.f, 0.f, 0.f};
#pragma unroll
      for (int i = 0; i < 4; ++i) acc = mfma16(wsel[i], of[i], acc);
      *(LAS v2u*)(L + a2w) = v2u{pk2(acc.x, acc.y), pk2(acc.z, acc.w)};
    }
    LBAR();
    if (w < 4) {
      v4u af[8];
#pragma unroll
      for (int ks = 0; ks < 8; ++ks) af[ks] = *(const LAS v4u*)(L + a2r + 64 * ks);
      SB();
      f4 acc = {0.f, 0.f, 0.f, 0.f};
#pragma unroll
      for (int ks = 0; ks < 8; ++ks) acc = mfma16(af[ks], wv[ks], acc);
      const float v = (g & 1) ? acc.z + acc.w : acc.x + acc.y;
      *(LAS u16*)(L + a3w) = (u16)pk2(v, v);
    }
    LBAR();
    {
      const v4u a0 = *(const LAS v4u*)(L + a3r), a1 = *(const LAS v4u*)(L + a3r + 64);
#pragma unroll
      for (int pi = 0; pi < 3; ++pi) {
        if (pi == 2 && w >= 4) break;
        f4 t0 = {0.f, 0.f, 0.f, 0.f}, t1 = {0.f, 0.f, 0.f, 0.f};
        t0 = mfma16(a0, wo[pi * 4 + 0], t0); t1 = mfma16(a0, wo[pi * 4 + 2], t1);
        t0 = mfma16(a1, wo[pi * 4 + 1], t0); t1 = mfma16(a1, wo[pi * 4 + 3], t1);
        sth1(guard(__float_as_uint((g & 1) ? t1.x : t0.x)), rw, po + pi * 1024);
      }
    }
    STAMP(9);
  } else if (bid < 192) {
    const int i2 = bid - 128;
    const int b = (i2 & 7) >> 2, ks = (i2 & 3) + 4 * (i2 >> 3), bk = b * 32 + ks;
    if (B_DELAY) __builtin_amdgcn_s_sleep(B_DELAY);
    {
      const u16* src = p.ckv + ((size_t)b * SK + 128 * ks) * DC;
#pragma unroll
      for (int n = 0; n < 16; ++n) {
        const int r0 = (w * 4 + (n >> 2)) * 4, blk = n & 3, row = r0 + g;
        dma(src + row * DC + 128 * blk + 8 * (r ^ swz(row)), L + blk * 32768 + r0 * 256);
      }
    }
    SB();
#pragma unroll
    for (int i = 0; i < 4; ++i) sth(sent4, rw, W_OP + bk * 32768 + (i * 512 + tid) * 16);
    if (tid < 16) sth(sent4, rw, W_ML + bk * 256 + tid * 16);
    const u32 ep = __builtin_amdgcn_raw_buffer_load_b32(rw, W_EPOCH, 0, SC1);
    STAMP(1);
    DRAIN();
    const u32 e1 = __builtin_amdgcn_readfirstlane(ep) + 1u;
    const v2u tag = {e1, e1 * 0x9E3779B1u ^ 0x5bd1e995u};
    asm volatile("" :: "v"(cpf));
    __builtin_amdgcn_s_barrier();
    CBAR();
    if (tid == 0) sth2(tag, rw, W_FLAG + (128 + bk) * 128);
    STAMP(2);
    v4u fr[32];
    const int hh = l >> 5;
    if (w < 4) {
      const int row = 32 * w + (l & 31), sw = swz(row);
#pragma unroll
      for (int s = 0; s < 32; ++s) fr[s] = *(const LAS v4u*)(L + (s >> 3) * 32768 + row * 256 + 16 * ((((2 * s) & 15) + hh) ^ sw));
    } else {
      const int bk2 = (l >> 4) & 1, q = (l & 15) >> 2, pp = l & 3;
#pragma unroll
      for (int ct = 0; ct < 4; ++ct)
#pragma unroll
        for (int kk = 0; kk < 8; ++kk) {
          const int r0 = 16 * kk + 4 * hh + q, r1 = r0 + 8, ch = 4 * ct + 2 * bk2 + (pp >> 1);
          LAS unsigned char* base = L + (w - 4) * 32768 + 8 * (pp & 1);
          fr[ct * 8 + kk] = trfrag(base + r0 * 256 + 16 * (ch ^ swz(r0)), base + r1 * 256 + 16 * (ch ^ swz(r1)));
        }
    }
    wait_flags(rw, 256 + (w + 8 * ((l & 31) >> 3)) * 8 + (l & 7), tag);
#pragma unroll
    for (int s = 0; s < 32; ++s) PIN(fr[s]);
    LBAR();
    STAMP(3);
    constexpr int L_MX = 33280, L_LS = 34304, L_P = 35328, L_SL = 43520;
    {
      v4u qv[4];
      const int o = W_QA + b * 32768 + tid * 16;
#pragma unroll 1
      for (u32 sp = 0; sp < SPIN_CAP; ++sp) {
        CBAR();
#pragma unroll
        for (int i = 0; i < 4; ++i) qv[i] = ldhs(rw, o, i * 8192);
        if (!__any(umax(umax(max4(qv[0]), max4(qv[1])), umax(max4(qv[2]), max4(qv[3]))) == SENT)) break;
      }
      STAMP(4);
#pragma unroll
      for (int i = 0; i < 4; ++i) *(LAS v4u*)(L + (w + 8 * i) * 1040 + l * 16) = qv[i];
    }
    LBAR();
    STAMP(5);
    if (w < 4) {
      f16v sacc;
#pragma unroll
      for (int i = 0; i < 16; ++i) sacc[i] = 0.f;
      const LAS unsigned char* qb = L + (l & 31) * 1040 + 16 * hh;
      v4u qf[12];
#pragma unroll
      for (int s = 0; s < 12; ++s) qf[s] = *(const LAS v4u*)(qb + 32 * s);
      SB();
#pragma unroll
      for (int s = 0; s < 32; ++s) {
        sacc = mfma32(fr[s], qf[s % 12], sacc);
        if (s + 12 < 32) qf[s % 12] = *(const LAS v4u*)(qb + 32 * (s + 12));
        SB();
      }
      float mx = sacc[0];
#pragma unroll
      for (int i = 1; i < 16; ++i) mx = fmaxf(mx, sacc[i]);
      *(LAS float*)(L + L_MX + ((l & 31) * 8 + w * 2 + hh) * 4) = mx;
      LBAR();
      STAMP(6);
      const f4 m0 = *(const LAS f4*)(L + L_MX + (l & 31) * 32), m1 = *(const LAS f4*)(L + L_MX + (l & 31) * 32 + 16);
      mx = fmaxf(fmaxf(fmaxf(m0.x, m0.y), fmaxf(m0.z, m0.w)), fmaxf(fmaxf(m1.x, m1.y), fmaxf(m1.z, m1.w))) * SCL;
      float e[16];
#pragma unroll
      for (int i = 0; i < 16; ++i) e[i] = __builtin_amdgcn_exp2f(__builtin_fmaf(sacc[i], SCL, -mx));
      *(LAS v4u*)(L + L_P + ((2 * w) * 64 + l) * 16) = v4u{pk2(e[0], e[1]), pk2(e[2], e[3]), pk2(e[4], e[5]), pk2(e[6], e[7])};
      *(LAS v4u*)(L + L_P + ((2 * w + 1) * 64 + l) * 16) = v4u{pk2(e[8], e[9]), pk2(e[10], e[11]), pk2(e[12], e[13]), pk2(e[14], e[15])};
      LBAR();
      STAMP(7);
      float sum = 0.f;
#pragma unroll
      for (int i = 0; i < 16; ++i) sum += e[i];
      *(LAS float*)(L + L_LS + ((l & 31) * 8 + w * 2 + hh) * 4) = sum;
      LBAR();
      if (w == 0 && l < 32) {
        const f4 s0 = *(const LAS f4*)(L + L_LS + l * 32), s1 = *(const LAS f4*)(L + L_LS + l * 32 + 16);
        sum = ((s0.x + s0.y) + (s0.z + s0.w)) + ((s1.x + s1.y) + (s1.z + s1.w));
        sth2(v2u{guard(__float_as_uint(mx)), guard(__float_as_uint(sum))}, rw, W_ML + bk * 256 + l * 8);
      }
    } else {
      LAS unsigned char* slab = L + L_SL + (w - 4) * 8704;
      int sw_ = (l & 31) * 272 + 8 * hh, sr_ = (l >> 3) * 272 + 16 * (l & 7);
      int oo = W_OP + bk * 32768 + (l >> 3) * 1024 + (128 * (w - 4) + 8 * (l & 7)) * 2;
      PIN(sw_); PIN(sr_); PIN(oo);
      __builtin_amdgcn_s_barrier();
      __builtin_amdgcn_s_barrier();
      CBAR();
      STAMP(7);
      v4u pf[8];
#pragma unroll
      for (int kk = 0; kk < 8; ++kk) pf[kk] = *(const LAS v4u*)(L + L_P + (kk * 64 + l) * 16);
      SB();
      f16v o[4];
#pragma unroll
      for (int ct = 0; ct < 4; ++ct) {
#pragma unroll
        for (int i = 0; i < 16; ++i) o[ct][i] = 0.f;
#pragma unroll
        for (int kk = 0; kk < 8; ++kk) o[ct] = mfma32(fr[ct * 8 + kk], pf[kk], o[ct]);
        if (ct == 0) __builtin_amdgcn_s_barrier();
#pragma unroll
        for (int g4 = 0; g4 < 4; ++g4)
          *(LAS v2u*)(slab + sw_ + (32 * ct + 8 * g4) * 2) = v2u{pk2(o[ct][4 * g4], o[ct][4 * g4 + 1]), pk2(o[ct][4 * g4 + 2], o[ct][4 * g4 + 3])};
        if (ct & 1) {
#pragma unroll
          for (int n = 0; n < 4; ++n) {
            const v4u v = *(const LAS v4u*)(slab + sr_ + n * 8 * 272 + 128 * (ct >> 1));
            __builtin_amdgcn_raw_buffer_store_b128(guard4(v), rw, oo + 128 * (ct >> 1), n * 8192, SC1);
          }
        }
      }
    }
    STAMP(8);
  } else {
    const int i3 = bid - 192;
    if (i3 >= 54) { asm volatile("" :: "v"(cpf)); return; }
    const u32 ep = __builtin_amdgcn_raw_buffer_load_b32(rw, W_EPOCH, 0, SC1);
    const u32 e1 = __builtin_amdgcn_readfirstlane(ep) + 1u;
    asm volatile("" :: "v"(cpf));
    const v2u tag = {e1, e1 * 0x9E3779B1u ^ 0x5bd1e995u};
    if (F_DELAY) __builtin_amdgcn_s_sleep(F_DELAY);
    if (i3 < 27) {
      STAMP(1);
      u32 xv[10];
#pragma unroll
      for (int f = 0; f < 10; ++f) xv[f] = __builtin_amdgcn_raw_buffer_load_b32(rx, (bsel * M + 80 * w + 20 * g) * 2 + 4 * f, 0, 0);
      v4u wf[20];
      const GAS u16* src = (const GAS u16*)((r >> 3) ? p.wu : p.wg) + (80 * w + 20 * g) * F + 64 * i3 + 8 * (r & 7);
#pragma unroll
      for (int f = 0; f < 20; ++f) wf[f] = __builtin_nontemporal_load((const GAS v4u*)(src + f * F));
      SB();
      f4 acc = {0.f, 0.f, 0.f, 0.f};
#pragma unroll
      for (int f = 0; f < 10; ++f) {
        acc = mfma16(selfrag(duplo(xv[f]), mask), wf[2 * f], acc);
        acc = mfma16(selfrag(duphi(xv[f]), mask), wf[2 * f + 1], acc);
      }
      *(LAS f4*)(L + (w * 64 + l) * 16) = acc;
      STAMP(2);
      LBAR();
      if (w == 0) {
        f4 s = *(const LAS f4*)(L + l * 16);
#pragma unroll
        for (int i = 1; i < 8; ++i) s += *(const LAS f4*)(L + (i * 64 + l) * 16);
        float hv[4];
#pragma unroll
        for (int i = 0; i < 4; ++i) { const float gs = s[i], us = ror8(s[i]); hv[i] = gs / (1.f + __expf(-gs)) * us; }
        if (r < 8) sth(v4u{pk2(hv[0], hv[1]), e1, pk2(hv[2], hv[3]), e1}, rw, W_H1 + i3 * 512 + (g >> 1) * 256 + (2 * r + (g & 1)) * 16);
      }
      STAMP(3);
    } else {
      const int u = i3 - 27;
      if (tid < 320) sth(sent4, rw, W_PF + u * 5120 + tid * 16);
      DRAIN();
      __builtin_amdgcn_s_barrier();
      if (tid == 0) sth2(tag, rw, W_FLAG + (192 + u) * 128);
      STAMP(1);
      if (w < 5) {
        const rsrc_t rd = mk(p.wd, F * M * 2);
        v4u wdn[16];
        const int vo = ((64 * u + 16 * g) * M + 128 * w + 8 * r) * 2;
#pragma unroll
        for (int f = 0; f < 16; ++f) wdn[f] = ldw(rd, vo, f * M * 2);
        SB();
        v4u z[4];
#pragma unroll 1
        for (u32 sp = 0; sp < SPIN_CAP; ++sp) {
          CBAR();
#pragma unroll
          for (int n = 0; n < 4; ++n) z[n] = ldh(rw, W_H1 + u * 512 + bsel * 256 + g * 64 + n * 16);
          u32 mn = e1, mx = e1;
#pragma unroll
          for (int n = 0; n < 4; ++n) { mn = umin(mn, umin(z[n].y, z[n].w)); mx = umax(mx, umax(z[n].y, z[n].w)); }
          if (!__any(mn != mx)) break;
          __builtin_amdgcn_s_sleep(2);
        }
        STAMP(2);
        f4 acc = {0.f, 0.f, 0.f, 0.f};
#pragma unroll
        for (int n = 0; n < 4; ++n) {
          acc = mfma16(selfrag(duplo(z[n].x), mask), wdn[4 * n], acc);
          acc = mfma16(selfrag(duphi(z[n].x), mask), wdn[4 * n + 1], acc);
          acc = mfma16(selfrag(duplo(z[n].z), mask), wdn[4 * n + 2], acc);
          acc = mfma16(selfrag(duphi(z[n].z), mask), wdn[4 * n + 3], acc);
        }
        sth(guard4(__builtin_bit_cast(v4u, acc)), rw, W_PF + u * 5120 + ((g >> 1) * M + 128 * w + 8 * r + 4 * (g & 1)) * 4);
      }
      STAMP(3);
    }
    if (i3 < 40) {
      const int du = i3, b = du / 20;
      float gate;
      {
        const rsrc_t rg = mk(p.gw, M * 2);
        float s = 0.f;
#pragma unroll
        for (int i = 0; i < 5; ++i) {
          const u32 xa = __builtin_amdgcn_raw_buffer_load_b32(rx, (b * M + 128 * i + 2 * l) * 2, 0, 0);
          const u32 ga = __builtin_amdgcn_raw_buffer_load_b32(rg, (128 * i + 2 * l) * 2, 0, 0);
          s += bflo(xa) * bflo(ga) + bfhi(xa) * bfhi(ga);
        }
#pragma unroll
        for (int o = 1; o < 64; o <<= 1) s += __shfl_xor(s, o);
        gate = 1.f / (1.f + __expf(-s));
      }
      const int pr = tid >> 3;
      wait_flags(rw, (l & 16) ? 192 + (int)umin(8 * w + (l & 7), 26) : 8 * w + (l & 7) + 8 * (l & 8), tag);
      f4 v = {0.f, 0.f, 0.f, 0.f};
      if (w < 4) {
        v4u t;
#pragma unroll 1
        for (u32 sp = 0; sp < SPIN_CAP; ++sp) {
          CBAR();
          t = ldh(rw, W_PF + (int)umin(pr, 26) * 5120 + du * 128 + (l & 7) * 16);
          if (!__any(max4(t) == SENT)) break;
          __builtin_amdgcn_s_sleep(2);
        }
        if (pr < 27) v = __builtin_bit_cast(f4, t) * (1.f - gate);
      }
      int lw = ((4 * (l & 7) + g) * 8 + w) * 4;
      PIN(lw); PIN(v);
      LAS unsigned char* LD = L + 16384;
      STAMP(9);
      {
        v4u t0, t1;
        const int o = W_PO + pr * 5120 + du * 128 + (l & 7) * 16;
#pragma unroll 1
        for (u32 sp = 0; sp < SPIN_CAP; ++sp) {
          CBAR();
          t0 = ldh(rw, o); t1 = ldhs(rw, o, 64 * 5120);
          if (!__any(umax(max4(t0), max4(t1)) == SENT)) break;
        }
        v += (__builtin_bit_cast(f4, t0) + __builtin_bit_cast(f4, t1)) * gate;
      }
      STAMP(10);
      float s = swap32add(swap16add(v.x, v.y), swap16add(v.z, v.w));
      s += ror8(s);
      *(LAS float*)(LD + lw) = s;
      LBAR();
      if (w == 0) {
        const f4 s0 = *(const LAS f4*)(LD + (l & 31) * 32), s1 = *(const LAS f4*)(LD + (l & 31) * 32 + 16);
        const float t = (((s0.x + s0.y) + (s0.z + s0.w)) + ((s1.x + s1.y) + (s1.z + s1.w)));
        if (l < 32) p.out[32 * du + l] = (u16)pk2(t, t);
        if (du == 0 && l == 0) sth1(e1, rw, W_EPOCH);
        if (du == 0) {
          const u64 now = __builtin_amdgcn_s_memrealtime();
          u64 mn = ~0ull, mx = 0;
          for (int i = 0; i < 4; ++i) { const u64 t = __hip_atomic_load((GAS u64*)(p.ws + W_DBG) + i * 64 + l, __ATOMIC_RELAXED, __HIP_MEMORY_SCOPE_AGENT); mn = t < mn ? t : mn; mx = t > mx ? t : mx; }
          for (int o = 1; o < 64; o <<= 1) { const u64 a = __shfl_xor(mn, o), b2 = __shfl_xor(mx, o); mn = a < mn ? a : mn; mx = b2 > mx ? b2 : mx; }
          u64 extra = DIAG == 1 ? (now - mn) : DIAG == 2 ? 4 * (mx - mn) : 0;
          if (extra > 10000) extra = 10000;
          while (__builtin_amdgcn_s_memrealtime() < now + extra) {}
        }
      }
      STAMP(11);
    }
  }
}

extern "C" void kernel_launch(void* const* d_in, const int* in_sizes, int n_in,
                              void* d_out, int out_size, void* d_ws, size_t ws_size,
                              hipStream_t stream) {
  Params p;
  p.x = (const u16*)d_in[0]; p.ckv = (const u16*)d_in[1]; p.wq = (const u16*)d_in[2]; p.wuk = (const u16*)d_in[3];
  p.wuv = (const u16*)d_in[4]; p.wo = (const u16*)d_in[5]; p.wg = (const u16*)d_in[7]; p.wu = (const u16*)d_in[8];
  p.wd = (const u16*)d_in[9]; p.gw = (const u16*)d_in[10]; p.out = (u16*)d_out; p.ws = (unsigned char*)d_ws;
  (void)hipFuncSetAttribute((const void*)mk_kernel, hipFuncAttributeMaxDynamicSharedMemorySize, LDS_BYTES);
  mk_kernel<<<256, NT, LDS_BYTES, stream>>>(p);
}
```

```cpp
#define GM 1
#include <hip/hip_runtime.h>
#include <stdint.h>

#define STAMPS 0
#define F_DELAY 30
#define B_DELAY 0

typedef unsigned short u16;
typedef unsigned int u32;
typedef unsigned long long u64;
typedef u32 v4u __attribute__((ext_vector_type(4)));
typedef u32 v2u __attribute__((ext_vector_type(2)));
typedef float f4 __attribute__((ext_vector_type(4)));
typedef float f2 __attribute__((ext_vector_type(2)));
typedef float f16v __attribute__((ext_vector_type(16)));
typedef short bf8 __attribute__((ext_vector_type(8)));
typedef short s4 __attribute__((ext_vector_type(4)));
typedef __bf16 bf2 __attribute__((ext_vector_type(2)));
#define GAS __attribute__((address_space(1)))
#define LAS __attribute__((address_space(3)))
#define DI __device__ __forceinline__
typedef __amdgpu_buffer_rsrc_t rsrc_t;

constexpr int NB = 2, H = 32, SK = 4096, D = 128, M = 640, DC = 512, F = 1728;
constexpr int NT = 512;
constexpr int W_EPOCH = 0;
constexpr int W_H1 = 4096;
constexpr int W_QA = 65536;
constexpr int W_ML = 131072;
constexpr int W_FLAG = 196608;
constexpr int W_QP = 262144;
constexpr int W_PO = 524288;
constexpr int W_PF = 1179648;
constexpr int W_OP = 2097152;
constexpr int W_END = 4194304;
constexpr int W_DBG = 8388608;
constexpr int LDS_BYTES = 131072 + 1024;
constexpr int SC1 = 16, NTL = 2;
constexpr u32 SENT = 0xFFFFFFFFu;
constexpr u32 SPIN_CAP = 1u << 20;
constexpr int CODE_PF_BYTES = 16384;
constexpr float SCL = 0.08838834764831845f * 1.4426950408889634f;

struct Params {
  const u16* x; const u16* ckv; const u16* wq; const u16* wuk; const u16* wuv; const u16* wo;
  const u16* wg; const u16* wu; const u16* wd; const u16* gw; u16* out; unsigned char* ws;
};

DI rsrc_t mk(const void* p, int bytes) { return __builtin_amdgcn_make_buffer_rsrc((void*)p, 0, bytes, 0x00020000); }
DI v4u ldw(rsrc_t r, int voff, int soff) { return __builtin_amdgcn_raw_buffer_load_b128(r, voff, soff, NTL); }
DI v4u ldh(rsrc_t r, int voff) { return __builtin_amdgcn_raw_buffer_load_b128(r, voff, 0, SC1); }
DI v4u ldhs(rsrc_t r, int voff, int soff) { return __builtin_amdgcn_raw_buffer_load_b128(r, voff, soff, SC1); }
DI v2u ldh2(rsrc_t r, int voff) { return __builtin_amdgcn_raw_buffer_load_b64(r, voff, 0, SC1); }
DI void sth(v4u v, rsrc_t r, int voff) { __builtin_amdgcn_raw_buffer_store_b128(v, r, voff, 0, SC1); }
DI void sth2(v2u v, rsrc_t r, int voff) { __builtin_amdgcn_raw_buffer_store_b64(v, r, voff, 0, SC1); }
DI void sth1(u32 v, rsrc_t r, int voff) { __builtin_amdgcn_raw_buffer_store_b32(v, r, voff, 0, SC1); }
DI u32 umin(u32 a, u32 b) { return a < b ? a : b; }
DI u32 umax(u32 a, u32 b) { return a > b ? a : b; }
DI u32 guard(u32 v) { return umin(v, 0xFFFFFFFEu); }
DI v4u guard4(v4u v) { return v4u{guard(v.x), guard(v.y), guard(v.z), guard(v.w)}; }
DI u32 max4(v4u v) { return umax(umax(v.x, v.y), umax(v.z, v.w)); }
DI u32 pk2(float lo, float hi) { bf2 r = {(__bf16)lo, (__bf16)hi}; return __builtin_bit_cast(u32, r); }
DI float bflo(u32 v) { return __uint_as_float(v << 16); }
DI float bfhi(u32 v) { return __uint_as_float(v & 0xFFFF0000u); }
DI f4 mfma16(v4u a, v4u b, f4 c) { return __builtin_amdgcn_mfma_f32_16x16x32_bf16(__builtin_bit_cast(bf8, a), __builtin_bit_cast(bf8, b), c, 0, 0, 0); }
DI f16v mfma32(v4u a, v4u b, f16v c) { return __builtin_amdgcn_mfma_f32_32x32x16_bf16(__builtin_bit_cast(bf8, a), __builtin_bit_cast(bf8, b), c, 0, 0, 0); }
#define CBAR() asm volatile("" ::: "memory")
#define SB() __builtin_amdgcn_sched_barrier(0)
#define PIN(x) asm volatile("" : "+v"(x))
#define DRAIN() asm volatile("s_waitcnt vmcnt(0)" ::: "memory")
#define LBAR() do { asm volatile("s_waitcnt lgkmcnt(0)" ::: "memory"); __builtin_amdgcn_s_barrier(); CBAR(); } while (0)
DI float ror8(float v) { return __builtin_bit_cast(float, __builtin_amdgcn_update_dpp(0, __builtin_bit_cast(int, v), 0x128, 0xf, 0xf, false)); }
DI float swap16add(float x, float y) { auto t = __builtin_amdgcn_permlane16_swap(__float_as_uint(x), __float_as_uint(y), false, false); return __uint_as_float(t[0]) + __uint_as_float(t[1]); }
DI float swap32add(float x, float y) { auto t = __builtin_amdgcn_permlane32_swap(__float_as_uint(x), __float_as_uint(y), false, false); return __uint_as_float(t[0]) + __uint_as_float(t[1]); }
DI v4u selfrag(u32 vv, v4u mask) { return v4u{vv & mask.x, vv & mask.y, vv & mask.z, vv & mask.w}; }
DI u32 duplo(u32 v) { return __builtin_amdgcn_perm(v, v, 0x01000100u); }
DI u32 duphi(u32 v) { return __builtin_amdgcn_perm(v, v, 0x03020302u); }
DI int swz(int row) { return ((row & 3) << 2) | ((row >> 2) & 3); }
DI void dma(const u16* src, LAS unsigned char* dst) { __builtin_amdgcn_global_load_lds((const GAS u32*)src, (LAS u32*)dst, 16, 0, NTL); }
DI v4u trfrag(LAS unsigned char* a0, LAS unsigned char* a1) {
  const v2u lo = __builtin_bit_cast(v2u, __builtin_amdgcn_ds_read_tr16_b64_v4i16((LAS s4*)a0));
  const v2u hi = __builtin_bit_cast(v2u, __builtin_amdgcn_ds_read_tr16_b64_v4i16((LAS s4*)a1));
  return v4u{lo.x, lo.y, hi.x, hi.y};
}

#define ROLE_OF(b) ((b) < 128 ? 0 : (b) < 192 ? 1 : ((b) < 246 ? 2 : -1))
#define STAMP(k) do {} while (0)

DI void wait_flags(rsrc_t rw, int idx, v2u tag) {
#pragma unroll 1
  for (u32 sp = 0; sp < SPIN_CAP; ++sp) {
    CBAR();
    v2u f = ldh2(rw, W_FLAG + idx * 128);
    if (!__any((f.x != tag.x) | (f.y != tag.y))) break;
    __builtin_amdgcn_s_sleep(1);
  }
  CBAR();
}

__global__ void __launch_bounds__(NT, 2) mk_kernel(Params p) {
  extern __shared__ __attribute__((aligned(16))) unsigned char smem[];
  LAS unsigned char* L = (LAS unsigned char*)smem;
  const int tid = threadIdx.x, l = tid & 63, w = __builtin_amdgcn_readfirstlane(tid >> 6);
  const int bid = blockIdx.x;
  const int r = l & 15, g = l >> 4;
  STAMP(0);
  { const u64 t0 = __builtin_amdgcn_s_memrealtime(); if (tid == 0) __hip_atomic_store((GAS u64*)(p.ws + W_DBG) + bid, t0, __ATOMIC_RELAXED, __HIP_MEMORY_SCOPE_AGENT); }
  u64 pc0; asm volatile("s_getpc_b64 %0" : "=s"(pc0));
  u32 cpf = 0;
  if (w >= 6) cpf = ((const GAS u32*)(pc0 & ~127ull))[umin(tid - 384, CODE_PF_BYTES / 128 - 1) * 32];
  CBAR(); SB();
  const rsrc_t rw = mk(p.ws, W_END);
  const rsrc_t rx = mk(p.x, NB * M * 2);
  const v4u sent4 = {SENT, SENT, SENT, SENT};
  const u32 m16 = (l & 1) ? 0xFFFF0000u : 0x0000FFFFu;
  const int jq = (l & 7) >> 1;
  const v4u mask = {jq == 0 ? m16 : 0u, jq == 1 ? m16 : 0u, jq == 2 ? m16 : 0u, jq == 3 ? m16 : 0u};
  const int bsel = (l >> 3) & 1;

  if (__builtin_expect(bid < 128, 1)) {
    const int h = (bid & 7) * 4 + (bid >> 5), j = (bid >> 3) & 3, ac = h * 4 + j;
    u32 e1;
    if (w < 2) {
      const rsrc_t rq = mk(p.wq, M * H * D * 2);
      const int mb = 160 * j + 80 * w + 20 * g;
      u32 xv[10];
#pragma unroll
      for (int f = 0; f < 10; ++f) xv[f] = __builtin_amdgcn_raw_buffer_load_b32(rx, (bsel * M + mb) * 2 + 4 * f, 0, 0);
      const u32 ep = __builtin_amdgcn_raw_buffer_load_b32(rw, W_EPOCH, 0, SC1);
      v4u wa[20];
      const int vo = (mb * (H * D) + h * D + 8 * r) * 2;
#pragma unroll
      for (int f = 0; f < 20; ++f) wa[f] = ldw(rq, vo, f * H * D * 2);
      SB();
      __builtin_amdgcn_s_barrier();
      SB();
      STAMP(1);
      v4u sel[20];
#pragma unroll
      for (int f = 0; f < 10; ++f) { sel[2 * f] = selfrag(duplo(xv[f]), mask); sel[2 * f + 1] = selfrag(duphi(xv[f]), mask); }
#pragma unroll
      for (int f = 0; f < 20; ++f) PIN(sel[f]);
      e1 = __builtin_amdgcn_readfirstlane(ep) + 1u;
      int o = W_QP + (ac * 2 + w) * 1024 + (g >> 1) * 512 + (r >> 2) * 128 + (g & 1) * 64 + (r & 3) * 16;
      PIN(o);
      SB();
      STAMP(2);
      f4 acc = {0.f, 0.f, 0.f, 0.f};
#pragma unroll
      for (int f = 0; f < 20; ++f) acc = mfma16(sel[f], wa[f], acc);
      sth(v4u{pk2(acc.x, acc.y), e1, pk2(acc.z, acc.w), e1}, rw, o);
      STAMP(3);
    } else if (w < 4) {
      const int w2 = w - 2;
      if (l < 16) sth(sent4, rw, W_QA + (((l >> 3) * H + h) * DC + 128 * j + 64 * w2) * 2 + (l & 7) * 16);
      const u32 ep = __builtin_amdgcn_raw_buffer_load_b32(rw, W_EPOCH, 0, SC1);
      const u16* src = p.wuk + ((size_t)(128 * j + 64 * w2 + g) * H + h) * D;
      LAS unsigned char* lk = L + w2 * 16384;
      __builtin_amdgcn_s_barrier();
      SB();
#pragma unroll
      for (int n = 0; n < 16; ++n) dma(src + n * 4 * H * D + 8 * (r ^ n), lk + n * 1024);
      DRAIN();
      STAMP(1);
      e1 = __builtin_amdgcn_readfirstlane(ep) + 1u;
      if (l == 0) sth2(v2u{e1, e1 * 0x9E3779B1u ^ 0x5bd1e995u}, rw, W_FLAG + (256 + ac * 2 + w2) * 128);
      v4u wk[16];
#pragma unroll
      for (int t = 0; t < 4; ++t)
#pragma unroll
        for (int s = 0; s < 4; ++s) wk[t * 4 + s] = *(const LAS v4u*)(lk + (4 * r + t) * 256 + 16 * ((4 * s + g) ^ r));
#pragma unroll
      for (int i = 0; i < 16; ++i) PIN(wk[i]);
      int oa = W_QA + (((g >> 1) * H + h) * DC + 128 * j + 64 * w2 + 4 * r + 2 * (g & 1)) * 2;
      PIN(oa);
      STAMP(2);
      v4u u[8];
      const int o = W_QP + ((h * 4 + ((r >> 1) & 3)) * 2 + (r & 1)) * 1024 + (r >> 3) * 512 + 16 * g;
#pragma unroll 1
      for (u32 sp = 0; sp < SPIN_CAP; ++sp) {
        CBAR();
#pragma unroll
        for (int n = 0; n < 8; ++n) u[n] = ldh(rw, o + 64 * n);
        u32 mn = e1, mx = e1;
#pragma unroll
        for (int n = 0; n < 8; ++n) { mn = umin(mn, umin(u[n].y, u[n].w)); mx = umax(mx, umax(u[n].y, u[n].w)); }
        if (!__any(mn != mx)) break;
      }
      STAMP(4);
      f4 a[4];
#pragma unroll
      for (int t = 0; t < 4; ++t) a[t] = f4{0.f, 0.f, 0.f, 0.f};
#pragma unroll
      for (int s = 0; s < 4; ++s) {
        const v4u qf = {u[2 * s].x, u[2 * s].z, u[2 * s + 1].x, u[2 * s + 1].z};
#pragma unroll
        for (int t = 0; t < 4; ++t) a[t] = mfma16(qf, wk[t * 4 + s], a[t]);
      }
      float st[4];
#pragma unroll
      for (int t = 0; t < 4; ++t) st[t] = (a[t].x + a[t].y) + (a[t].z + a[t].w);
      sth1(guard(pk2(swap16add(st[0], st[2]), swap16add(st[1], st[3]))), rw, oa);
      STAMP(5);
    } else {
      const u32 ep = __builtin_amdgcn_raw_buffer_load_b32(rw, W_EPOCH, 0, SC1);
      if (w == 4) {
#pragma unroll
        for (int i = 0; i < 5; ++i) sth(sent4, rw, W_PO + ac * 5120 + i * 1024 + l * 16);
      }
      __builtin_amdgcn_s_barrier();
      DRAIN();
      e1 = __builtin_amdgcn_readfirstlane(ep) + 1u;
      if (w == 4 && l == 0) sth2(v2u{e1, e1 * 0x9E3779B1u ^ 0x5bd1e995u}, rw, W_FLAG + ac * 128);
      asm volatile("" :: "v"(cpf));
      STAMP(1);
    }
    const v2u tag = {e1, e1 * 0x9E3779B1u ^ 0x5bd1e995u};
    SB();
    __builtin_amdgcn_s_barrier();
    SB();
    const int cj = j >> 1, dj = j & 1;
    {
      const u16* sv = p.wuv + ((size_t)(256 * cj + 32 * w + (l >> 3)) * H + h) * D + 64 * dj;
#pragma unroll
      for (int n = 0; n < 4; ++n) {
        const int fz = 2 * (((l >> 4) & 1) | ((n & 1) << 1));
        dma(sv + n * 8 * H * D + 8 * ((l & 7) ^ fz), L + (w * 4 + n) * 1024);
      }
      const u16* so = p.wo + ((size_t)h * D + 64 * dj + 8 * w + g) * M;
      const int fo = 2 * (g | ((w & 1) << 2));
#pragma unroll
      for (int n = 0; n < 10; ++n) dma(so + (n & 1) * 4 * M + (n >> 1) * 128 + 8 * (r ^ fo), L + 32768 + (n >> 1) * 16384 + (8 * w + 4 * (n & 1)) * 256);
    }
    const int cb_b = w >> 2, cb = (w >> 1) & 1, kh = w & 1;
    DRAIN();
    __builtin_amdgcn_s_barrier();
    CBAR();
    STAMP(6);
    v4u wv[8], wo[12];
    {
      const int q = r >> 2, pp = l & 3;
#pragma unroll
      for (int ks = 0; ks < 8; ++ks) {
        const int row = 32 * ks + 8 * g + q, fz = 2 * (((q >> 1) & 1) | ((g & 1) << 1));
        LAS unsigned char* a = L + row * 128 + 16 * ((2 * (w & 3) + (pp >> 1)) ^ fz) + 8 * (pp & 1);
        wv[ks] = trfrag(a, a + 512);
      }
#pragma unroll
      for (int pi = 0; pi < 3; ++pi) {
        const int P = (pi == 2 ? (w & 3) : w) + 8 * pi;
#pragma unroll
        for (int tt = 0; tt < 2; ++tt)
#pragma unroll
          for (int ks = 0; ks < 2; ++ks) {
            const int row = 32 * ks + 8 * g + q, fo = 2 * (q | ((g & 1) << 2));
            LAS unsigned char* a = L + 32768 + (P >> 2) * 16384 + row * 256 + 16 * ((4 * (P & 3) + 2 * tt + (pp >> 1)) ^ fo) + 8 * (pp & 1);
            wo[pi * 4 + tt * 2 + ks] = trfrag(a, a + 1024);
          }
      }
#pragma unroll
      for (int i = 0; i < 8; ++i) PIN(wv[i]);
#pragma unroll
      for (int i = 0; i < 12; ++i) PIN(wo[i]);
    }
    wait_flags(rw, 128 + cb_b * 32 + (l & 31), tag);
    v4u wsel[4];
    {
      v2u ml;
#pragma unroll 1
      for (u32 sp = 0; sp < SPIN_CAP; ++sp) {
        CBAR();
        ml = ldh2(rw, W_ML + ((cb_b * 32 + (l & 31)) * H + h) * 8);
        if (!__any(umax(ml.x, ml.y) == SENT)) break;
      }
      const float mm = __uint_as_float(ml.x), ll = __uint_as_float(ml.y);
      float mx = mm;
#pragma unroll
      for (int o = 1; o < 32; o <<= 1) mx = fmaxf(mx, __shfl_xor(mx, o));
      const float e = __builtin_amdgcn_exp2f(mm - mx);
      float sum = e * ll;
#pragma unroll
      for (int o = 1; o < 32; o <<= 1) sum += __shfl_xor(sum, o);
      const float wgt = e * __builtin_amdgcn_rcpf(sum);
      const u32 wb = pk2(wgt, wgt);
#pragma unroll
      for (int i = 0; i < 4; ++i) { wsel[i] = selfrag(__shfl(wb, 16 * kh + 4 * i + g), mask); PIN(wsel[i]); }
    }
    constexpr int L_A2 = 114688, L_A3 = L_A2 + 4 * 528;
    int a2w = L_A2 + (cb_b * 2 + kh) * 528 + (128 * cb + 8 * r + 4 * (g & 1)) * 2;
    int a2r = L_A2 + (r & 3) * 528 + 16 * g;
    int a3w = L_A3 + (g & 1) * 144 + (16 * (w & 3) + r) * 2;
    int a3r = L_A3 + (r >> 3) * 144 + 16 * g;
    int po = W_PO + ac * 5120 + ((g >> 1) * M + 32 * w + 16 * (g & 1) + r) * 4;
    PIN(a2w); PIN(a2r); PIN(a3w); PIN(a3r); PIN(po);
    STAMP(7);
    v4u of[4];
    {
      const int o = W_OP + (((cb_b * 32 + 16 * kh + g) * H + h) * DC + 256 * cj + 128 * cb + 8 * r) * 2;
#pragma unroll 1
      for (u32 sp = 0; sp < SPIN_CAP; ++sp) {
        CBAR();
#pragma unroll
        for (int i = 0; i < 4; ++i) of[i] = ldhs(rw, o, i * 4 * H * DC * 2);
        if (!__any(umax(umax(max4(of[0]), max4(of[1])), umax(max4(of[2]), max4(of[3]))) == SENT)) break;
      }
    }
    STAMP(8);
    {
      f4 acc = {0.f, 0.f, 0.f, 0.f};
#pragma unroll
      for (int i = 0; i < 4; ++i) acc = mfma16(wsel[i], of[i], acc);
      *(LAS v2u*)(L + a2w) = v2u{pk2(acc.x, acc.y), pk2(acc.z, acc.w)};
    }
    LBAR();
    if (w < 4) {
      v4u af[8];
#pragma unroll
      for (int ks = 0; ks < 8; ++ks) af[ks] = *(const LAS v4u*)(L + a2r + 64 * ks);
      SB();
      f4 acc = {0.f, 0.f, 0.f, 0.f};
#pragma unroll
      for (int ks = 0; ks < 8; ++ks) acc = mfma16(af[ks], wv[ks], acc);
      const float v = (g & 1) ? acc.z + acc.w : acc.x + acc.y;
      *(LAS u16*)(L + a3w) = (u16)pk2(v, v);
    }
    LBAR();
    {
      const v4u a0 = *(const LAS v4u*)(L + a3r), a1 = *(const LAS v4u*)(L + a3r + 64);
#pragma unroll
      for (int pi = 0; pi < 3; ++pi) {
        if (pi == 2 && w >= 4) break;
        f4 t0 = {0.f, 0.f, 0.f, 0.f}, t1 = {0.f, 0.f, 0.f, 0.f};
        t0 = mfma16(a0, wo[pi * 4 + 0], t0); t1 = mfma16(a0, wo[pi * 4 + 2], t1);
        t0 = mfma16(a1, wo[pi * 4 + 1], t0); t1 = mfma16(a1, wo[pi * 4 + 3], t1);
        sth1(guard(__float_as_uint((g & 1) ? t1.x : t0.x)), rw, po + pi * 1024);
      }
    }
    STAMP(9);
  } else if (bid < 192) {
    const int i2 = bid - 128;
    const int b = (i2 & 7) >> 2, ks = (i2 & 3) + 4 * (i2 >> 3), bk = b * 32 + ks;
    if (B_DELAY) __builtin_amdgcn_s_sleep(B_DELAY);
    {
      const u16* src = p.ckv + ((size_t)b * SK + 128 * ks) * DC;
#pragma unroll
      for (int n = 0; n < 16; ++n) {
        const int r0 = (w * 4 + (n >> 2)) * 4, blk = n & 3, row = r0 + g;
        dma(src + row * DC + 128 * blk + 8 * (r ^ swz(row)), L + blk * 32768 + r0 * 256);
      }
    }
    SB();
#pragma unroll
    for (int i = 0; i < 4; ++i) sth(sent4, rw, W_OP + bk * 32768 + (i * 512 + tid) * 16);
    if (tid < 16) sth(sent4, rw, W_ML + bk * 256 + tid * 16);
    const u32 ep = __builtin_amdgcn_raw_buffer_load_b32(rw, W_EPOCH, 0, SC1);
    STAMP(1);
    DRAIN();
    const u32 e1 = __builtin_amdgcn_readfirstlane(ep) + 1u;
    const v2u tag = {e1, e1 * 0x9E3779B1u ^ 0x5bd1e995u};
    asm volatile("" :: "v"(cpf));
    __builtin_amdgcn_s_barrier();
    CBAR();
    if (tid == 0) sth2(tag, rw, W_FLAG + (128 + bk) * 128);
    STAMP(2);
    v4u fr[32];
    const int hh = l >> 5;
    if (w < 4) {
      const int row = 32 * w + (l & 31), sw = swz(row);
#pragma unroll
      for (int s = 0; s < 32; ++s) fr[s] = *(const LAS v4u*)(L + (s >> 3) * 32768 + row * 256 + 16 * ((((2 * s) & 15) + hh) ^ sw));
    } else {
      const int bk2 = (l >> 4) & 1, q = (l & 15) >> 2, pp = l & 3;
#pragma unroll
      for (int ct = 0; ct < 4; ++ct)
#pragma unroll
        for (int kk = 0; kk < 8; ++kk) {
          const int r0 = 16 * kk + 4 * hh + q, r1 = r0 + 8, ch = 4 * ct + 2 * bk2 + (pp >> 1);
          LAS unsigned char* base = L + (w - 4) * 32768 + 8 * (pp & 1);
          fr[ct * 8 + kk] = trfrag(base + r0 * 256 + 16 * (ch ^ swz(r0)), base + r1 * 256 + 16 * (ch ^ swz(r1)));
        }
    }
    wait_flags(rw, 256 + (w + 8 * ((l & 31) >> 3)) * 8 + (l & 7), tag);
#pragma unroll
    for (int s = 0; s < 32; ++s) PIN(fr[s]);
    LBAR();
    STAMP(3);
    constexpr int L_MX = 33280, L_LS = 34304, L_P = 35328, L_SL = 43520;
    {
      v4u qv[4];
      const int o = W_QA + b * 32768 + tid * 16;
#pragma unroll 1
      for (u32 sp = 0; sp < SPIN_CAP; ++sp) {
        CBAR();
#pragma unroll
        for (int i = 0; i < 4; ++i) qv[i] = ldhs(rw, o, i * 8192);
        if (!__any(umax(umax(max4(qv[0]), max4(qv[1])), umax(max4(qv[2]), max4(qv[3]))) == SENT)) break;
      }
      STAMP(4);
#pragma unroll
      for (int i = 0; i < 4; ++i) *(LAS v4u*)(L + (w + 8 * i) * 1040 + l * 16) = qv[i];
    }
    LBAR();
    STAMP(5);
    if (w < 4) {
      f16v sacc;
#pragma unroll
      for (int i = 0; i < 16; ++i) sacc[i] = 0.f;
      const LAS unsigned char* qb = L + (l & 31) * 1040 + 16 * hh;
      v4u qf[12];
#pragma unroll
      for (int s = 0; s < 12; ++s) qf[s] = *(const LAS v4u*)(qb + 32 * s);
      SB();
#pragma unroll
      for (int s = 0; s < 32; ++s) {
        sacc = mfma32(fr[s], qf[s % 12], sacc);
        if (s + 12 < 32) qf[s % 12] = *(const LAS v4u*)(qb + 32 * (s + 12));
        SB();
      }
      float mx = sacc[0];
#pragma unroll
      for (int i = 1; i < 16; ++i) mx = fmaxf(mx, sacc[i]);
      *(LAS float*)(L + L_MX + ((l & 31) * 8 + w * 2 + hh) * 4) = mx;
      LBAR();
      STAMP(6);
      const f4 m0 = *(const LAS f4*)(L + L_MX + (l & 31) * 32), m1 = *(const LAS f4*)(L + L_MX + (l & 31) * 32 + 16);
      mx = fmaxf(fmaxf(fmaxf(m0.x, m0.y), fmaxf(m0.z, m0.w)), fmaxf(fmaxf(m1.x, m1.y), fmaxf(m1.z, m1.w))) * SCL;
      float e[16];
#pragma unroll
      for (int i = 0; i < 16; ++i) e[i] = __builtin_amdgcn_exp2f(__builtin_fmaf(sacc[i], SCL, -mx));
      *(LAS v4u*)(L + L_P + ((2 * w) * 64 + l) * 16) = v4u{pk2(e[0], e[1]), pk2(e[2], e[3]), pk2(e[4], e[5]), pk2(e[6], e[7])};
      *(LAS v4u*)(L + L_P + ((2 * w + 1) * 64 + l) * 16) = v4u{pk2(e[8], e[9]), pk2(e[10], e[11]), pk2(e[12], e[13]), pk2(e[14], e[15])};
      LBAR();
      STAMP(7);
      float sum = 0.f;
#pragma unroll
      for (int i = 0; i < 16; ++i) sum += e[i];
      *(LAS float*)(L + L_LS + ((l & 31) * 8 + w * 2 + hh) * 4) = sum;
      LBAR();
      if (w == 0 && l < 32) {
        const f4 s0 = *(const LAS f4*)(L + L_LS + l * 32), s1 = *(const LAS f4*)(L + L_LS + l * 32 + 16);
        sum = ((s0.x + s0.y) + (s0.z + s0.w)) + ((s1.x + s1.y) + (s1.z + s1.w));
        sth2(v2u{guard(__float_as_uint(mx)), guard(__float_as_uint(sum))}, rw, W_ML + bk * 256 + l * 8);
      }
    } else {
      LAS unsigned char* slab = L + L_SL + (w - 4) * 8704;
      int sw_ = (l & 31) * 272 + 8 * hh, sr_ = (l >> 3) * 272 + 16 * (l & 7);
      int oo = W_OP + bk * 32768 + (l >> 3) * 1024 + (128 * (w - 4) + 8 * (l & 7)) * 2;
      PIN(sw_); PIN(sr_); PIN(oo);
      __builtin_amdgcn_s_barrier();
      __builtin_amdgcn_s_barrier();
      CBAR();
      STAMP(7);
      v4u pf[8];
#pragma unroll
      for (int kk = 0; kk < 8; ++kk) pf[kk] = *(const LAS v4u*)(L + L_P + (kk * 64 + l) * 16);
      SB();
      f16v o[4];
#pragma unroll
      for (int ct = 0; ct < 4; ++ct) {
#pragma unroll
        for (int i = 0; i < 16; ++i) o[ct][i] = 0.f;
#pragma unroll
        for (int kk = 0; kk < 8; ++kk) o[ct] = mfma32(fr[ct * 8 + kk], pf[kk], o[ct]);
        if (ct == 0) __builtin_amdgcn_s_barrier();
#pragma unroll
        for (int g4 = 0; g4 < 4; ++g4)
          *(LAS v2u*)(slab + sw_ + (32 * ct + 8 * g4) * 2) = v2u{pk2(o[ct][4 * g4], o[ct][4 * g4 + 1]), pk2(o[ct][4 * g4 + 2], o[ct][4 * g4 + 3])};
        if (ct & 1) {
#pragma unroll
          for (int n = 0; n < 4; ++n) {
            const v4u v = *(const LAS v4u*)(slab + sr_ + n * 8 * 272 + 128 * (ct >> 1));
            __builtin_amdgcn_raw_buffer_store_b128(guard4(v), rw, oo + 128 * (ct >> 1), n * 8192, SC1);
          }
        }
      }
    }
    STAMP(8);
  } else {
    const int i3 = bid - 192;
    if (i3 >= 54) { asm volatile("" :: "v"(cpf)); return; }
    const u32 ep = __builtin_amdgcn_raw_buffer_load_b32(rw, W_EPOCH, 0, SC1);
    const u32 e1 = __builtin_amdgcn_readfirstlane(ep) + 1u;
    asm volatile("" :: "v"(cpf));
    const v2u tag = {e1, e1 * 0x9E3779B1u ^ 0x5bd1e995u};
    if (F_DELAY) __builtin_amdgcn_s_sleep(F_DELAY);
    if (i3 < 27) {
      STAMP(1);
      u32 xv[10];
#pragma unroll
      for (int f = 0; f < 10; ++f) xv[f] = __builtin_amdgcn_raw_buffer_load_b32(rx, (bsel * M + 80 * w + 20 * g) * 2 + 4 * f, 0, 0);
      v4u wf[20];
      const GAS u16* src = (const GAS u16*)((r >> 3) ? p.wu : p.wg) + (80 * w + 20 * g) * F + 64 * i3 + 8 * (r & 7);
#pragma unroll
      for (int f = 0; f < 20; ++f) wf[f] = __builtin_nontemporal_load((const GAS v4u*)(src + f * F));
      SB();
      f4 acc = {0.f, 0.f, 0.f, 0.f};
#pragma unroll
      for (int f = 0; f < 10; ++f) {
        acc = mfma16(selfrag(duplo(xv[f]), mask), wf[2 * f], acc);
        acc = mfma16(selfrag(duphi(xv[f]), mask), wf[2 * f + 1], acc);
      }
      *(LAS f4*)(L + (w * 64 + l) * 16) = acc;
      STAMP(2);
      LBAR();
      if (w == 0) {
        f4 s = *(const LAS f4*)(L + l * 16);
#pragma unroll
        for (int i = 1; i < 8; ++i) s += *(const LAS f4*)(L + (i * 64 + l) * 16);
        float hv[4];
#pragma unroll
        for (int i = 0; i < 4; ++i) { const float gs = s[i], us = ror8(s[i]); hv[i] = gs / (1.f + __expf(-gs)) * us; }
        if (r < 8) sth(v4u{pk2(hv[0], hv[1]), e1, pk2(hv[2], hv[3]), e1}, rw, W_H1 + i3 * 512 + (g >> 1) * 256 + (2 * r + (g & 1)) * 16);
      }
      STAMP(3);
    } else {
      const int u = i3 - 27;
      if (tid < 320) sth(sent4, rw, W_PF + u * 5120 + tid * 16);
      DRAIN();
      __builtin_amdgcn_s_barrier();
      if (tid == 0) sth2(tag, rw, W_FLAG + (192 + u) * 128);
      STAMP(1);
      if (w < 5) {
        const rsrc_t rd = mk(p.wd, F * M * 2);
        v4u wdn[16];
        const int vo = ((64 * u + 16 * g) * M + 128 * w + 8 * r) * 2;
#pragma unroll
        for (int f = 0; f < 16; ++f) wdn[f] = ldw(rd, vo, f * M * 2);
        SB();
        v4u z[4];
#pragma unroll 1
        for (u32 sp = 0; sp < SPIN_CAP; ++sp) {
          CBAR();
#pragma unroll
          for (int n = 0; n < 4; ++n) z[n] = ldh(rw, W_H1 + u * 512 + bsel * 256 + g * 64 + n * 16);
          u32 mn = e1, mx = e1;
#pragma unroll
          for (int n = 0; n < 4; ++n) { mn = umin(mn, umin(z[n].y, z[n].w)); mx = umax(mx, umax(z[n].y, z[n].w)); }
          if (!__any(mn != mx)) break;
          __builtin_amdgcn_s_sleep(2);
        }
        STAMP(2);
        f4 acc = {0.f, 0.f, 0.f, 0.f};
#pragma unroll
        for (int n = 0; n < 4; ++n) {
          acc = mfma16(selfrag(duplo(z[n].x), mask), wdn[4 * n], acc);
          acc = mfma16(selfrag(duphi(z[n].x), mask), wdn[4 * n + 1], acc);
          acc = mfma16(selfrag(duplo(z[n].z), mask), wdn[4 * n + 2], acc);
          acc = mfma16(selfrag(duphi(z[n].z), mask), wdn[4 * n + 3], acc);
        }
        sth(guard4(__builtin_bit_cast(v4u, acc)), rw, W_PF + u * 5120 + ((g >> 1) * M + 128 * w + 8 * r + 4 * (g & 1)) * 4);
      }
      STAMP(3);
    }
    if (i3 < 40) {
      const int du = i3, b = du / 20;
      float gate;
      {
        const rsrc_t rg = mk(p.gw, M * 2);
        float s = 0.f;
#pragma unroll
        for (int i = 0; i < 5; ++i) {
          const u32 xa = __builtin_amdgcn_raw_buffer_load_b32(rx, (b * M + 128 * i + 2 * l) * 2, 0, 0);
          const u32 ga = __builtin_amdgcn_raw_buffer_load_b32(rg, (128 * i + 2 * l) * 2, 0, 0);
          s += bflo(xa) * bflo(ga) + bfhi(xa) * bfhi(ga);
        }
#pragma unroll
        for (int o = 1; o < 64; o <<= 1) s += __shfl_xor(s, o);
        gate = 1.f / (1.f + __expf(-s));
      }
      const int pr = tid >> 3;
      wait_flags(rw, (l & 16) ? 192 + (int)umin(8 * w + (l & 7), 26) : 8 * w + (l & 7) + 8 * (l & 8), tag);
      f4 v = {0.f, 0.f, 0.f, 0.f};
      if (w < 4) {
        v4u t;
#pragma unroll 1
        for (u32 sp = 0; sp < SPIN_CAP; ++sp) {
          CBAR();
          t = ldh(rw, W_PF + (int)umin(pr, 26) * 5120 + du * 128 + (l & 7) * 16);
          if (!__any(max4(t) == SENT)) break;
          __builtin_amdgcn_s_sleep(2);
        }
        if (pr < 27) v = __builtin_bit_cast(f4, t) * (1.f - gate);
      }
      int lw = ((4 * (l & 7) + g) * 8 + w) * 4;
      PIN(lw); PIN(v);
      LAS unsigned char* LD = L + 16384;
      STAMP(9);
      {
        v4u t0, t1;
        const int o = W_PO + pr * 5120 + du * 128 + (l & 7) * 16;
#pragma unroll 1
        for (u32 sp = 0; sp < SPIN_CAP; ++sp) {
          CBAR();
          t0 = ldh(rw, o); t1 = ldhs(rw, o, 64 * 5120);
          if (!__any(umax(max4(t0), max4(t1)) == SENT)) break;
        }
        v += (__builtin_bit_cast(f4, t0) + __builtin_bit_cast(f4, t1)) * gate;
      }
      STAMP(10);
      float s = swap32add(swap16add(v.x, v.y), swap16add(v.z, v.w));
      s += ror8(s);
      *(LAS float*)(LD + lw) = s;
      LBAR();
      if (w == 0) {
        const f4 s0 = *(const LAS f4*)(LD + (l & 31) * 32), s1 = *(const LAS f4*)(LD + (l & 31) * 32 + 16);
        const float t = (((s0.x + s0.y) + (s0.z + s0.w)) + ((s1.x + s1.y) + (s1.z + s1.w)));
        if (l < 32) p.out[32 * du + l] = (u16)pk2(t, t);
        if (du == 0 && l == 0) sth1(e1, rw, W_EPOCH);
        if (l == 0) __hip_atomic_store((GAS u64*)(p.ws + W_DBG) + 256 + du, (u64)__builtin_amdgcn_s_memrealtime(), __ATOMIC_RELAXED, __HIP_MEMORY_SCOPE_AGENT);
      }
      STAMP(11);
    }
  }
}

__global__ void k0(unsigned char* ws) { if (threadIdx.x == 0) ((u64*)(ws + W_DBG))[512] = __builtin_amdgcn_s_memrealtime(); }
__global__ void k2(unsigned char* ws) {
  const u64 now = __builtin_amdgcn_s_memrealtime();
  const int l = threadIdx.x;
  const u64* d = (const u64*)(ws + W_DBG);
  u64 mn = ~0ull, mx = 0;
  for (int i = 0; i < 4; ++i) { const u64 t = d[i * 64 + l]; mn = t < mn ? t : mn; }
  if (l < 40) mx = d[256 + l];
  for (int o = 1; o < 64; o <<= 1) { const u64 a = __shfl_xor(mn, o), b2 = __shfl_xor(mx, o); mn = a < mn ? a : mn; mx = b2 > mx ? b2 : mx; }
  u64 extra = GM == 2 ? 4 * (now - mx) : GM == 3 ? 4 * (mn - d[512]) : 0;
  if (extra > 10000) extra = 10000;
  while (__builtin_amdgcn_s_memrealtime() < now + extra) {}
}
extern "C" void kernel_launch(void* const* d_in, const int* in_sizes, int n_in,
                              void* d_out, int out_size, void* d_ws, size_t ws_size,
                              hipStream_t stream) {
  Params p;
  p.x = (const u16*)d_in[0]; p.ckv = (const u16*)d_in[1]; p.wq = (const u16*)d_in[2]; p.wuk = (const u16*)d_in[3];
  p.wuv = (const u16*)d_in[4]; p.wo = (const u16*)d_in[5]; p.wg = (const u16*)d_in[7]; p.wu = (const u16*)d_in[8];
  p.wd = (const u16*)d_in[9]; p.gw = (const u16*)d_in[10]; p.out = (u16*)d_out; p.ws = (unsigned char*)d_ws;
  (void)hipFuncSetAttribute((const void*)mk_kernel, hipFuncAttributeMaxDynamicSharedMemorySize, LDS_BYTES);
  if (GM >= 3) k0<<<1, 64, 0, stream>>>(p.ws);
  mk_kernel<<<256, NT, LDS_BYTES, stream>>>(p);
  k2<<<1, 64, 0, stream>>>(p.ws);
}
```

```cpp
#include <hip/hip_runtime.h>
#include <stdint.h>

#define STAMPS 0
#define WS_OFF(n) 0
#define TUNE 0
#define KNOB(i) kKnob[i]
#define KSET 0, 0, 5, 0, 0, 2, 0, 0, 0, 0, 0, 0
constexpr int kKnob[12] = {KSET};

typedef unsigned short u16;
typedef unsigned int u32;
typedef unsigned long long u64;
typedef u32 v4u __attribute__((ext_vector_type(4)));
typedef u32 v2u __attribute__((ext_vector_type(2)));
typedef float f4 __attribute__((ext_vector_type(4)));
typedef float f2 __attribute__((ext_vector_type(2)));
typedef float f16v __attribute__((ext_vector_type(16)));
typedef short bf8 __attribute__((ext_vector_type(8)));
typedef short s4 __attribute__((ext_vector_type(4)));
typedef __bf16 bf2 __attribute__((ext_vector_type(2)));
#define GAS __attribute__((address_space(1)))
#define LAS __attribute__((address_space(3)))
#define DI __device__ __forceinline__
typedef __amdgpu_buffer_rsrc_t rsrc_t;

constexpr int NB = 2, H = 32, SK = 4096, D = 128, M = 640, DC = 512, F = 1728;
constexpr int NT = 512;
constexpr int W_EPOCH = 0;
constexpr int W_FLAG = 4096;
constexpr int W_QA = 65536;
constexpr int W_ML = 24576;
constexpr int W_FX = 131072;
constexpr int W_QP = 262144;
constexpr int W_PO = 524288;
constexpr int W_PF = 1179648;
constexpr int W_OP = 2097152;
constexpr int W_END = 4194304;
constexpr int W_DBG = 8388608;
constexpr int LDS_BYTES = 163840;
constexpr int SC1 = 16, NTL = 2;
constexpr u32 SENT = 0xFFFFFFFFu;
constexpr u32 SPIN_CAP = 1u << 20;
#define CODE_PF 16896
constexpr int CODE_PF_BYTES = CODE_PF;
constexpr float SCL = 0.08838834764831845f * 1.4426950408889634f;

struct Params {
  const u16* x; const u16* ckv; const u16* wq; const u16* wuk; const u16* wuv; const u16* wo;
  const u16* wg; const u16* wu; const u16* wd; const u16* gw; u16* out; unsigned char* ws;
};

DI rsrc_t mk(const void* p, int bytes) { return __builtin_amdgcn_make_buffer_rsrc((void*)p, 0, bytes, 0x00020000); }
DI v4u ldw(rsrc_t r, int voff, int soff) { return __builtin_amdgcn_raw_buffer_load_b128(r, voff, soff, NTL); }
DI v4u ldh(rsrc_t r, int voff) { return __builtin_amdgcn_raw_buffer_load_b128(r, voff, 0, SC1); }
DI v4u ldhs(rsrc_t r, int voff, int soff) { return __builtin_amdgcn_raw_buffer_load_b128(r, voff, soff, SC1); }
DI v2u ldh2(rsrc_t r, int voff) { return __builtin_amdgcn_raw_buffer_load_b64(r, voff, 0, SC1); }
DI void sth(v4u v, rsrc_t r, int voff) { __builtin_amdgcn_raw_buffer_store_b128(v, r, voff, 0, SC1); }
DI void sths(v4u v, rsrc_t r, int voff, int soff) { __builtin_amdgcn_raw_buffer_store_b128(v, r, voff, soff, SC1); }
DI void sth2(v2u v, rsrc_t r, int voff) { __builtin_amdgcn_raw_buffer_store_b64(v, r, voff, 0, SC1); }
DI void sth1(u32 v, rsrc_t r, int voff) { __builtin_amdgcn_raw_buffer_store_b32(v, r, voff, 0, SC1); }
DI u32 umin(u32 a, u32 b) { return a < b ? a : b; }
DI u32 umax(u32 a, u32 b) { return a > b ? a : b; }
DI u32 guard(u32 v) { return umin(v, 0xFFFFFFFEu); }
DI v4u guard4(v4u v) { return v4u{guard(v.x), guard(v.y), guard(v.z), guard(v.w)}; }
DI u32 max4(v4u v) { return umax(umax(v.x, v.y), umax(v.z, v.w)); }
DI u32 pk2(float lo, float hi) { bf2 r = {(__bf16)lo, (__bf16)hi}; return __builtin_bit_cast(u32, r); }
DI float bflo(u32 v) { return __uint_as_float(v << 16); }
DI float bfhi(u32 v) { return __uint_as_float(v & 0xFFFF0000u); }
DI f4 mfma16(v4u a, v4u b, f4 c) { return __builtin_amdgcn_mfma_f32_16x16x32_bf16(__builtin_bit_cast(bf8, a), __builtin_bit_cast(bf8, b), c, 0, 0, 0); }
DI f16v mfma32(v4u a, v4u b, f16v c) { return __builtin_amdgcn_mfma_f32_32x32x16_bf16(__builtin_bit_cast(bf8, a), __builtin_bit_cast(bf8, b), c, 0, 0, 0); }
#define CBAR() asm volatile("" ::: "memory")
#define SB() __builtin_amdgcn_sched_barrier(0)
#define PIN(x) asm volatile("" : "+v"(x))
#define DRAIN() asm volatile("s_waitcnt vmcnt(0)" ::: "memory")
#define VMWAIT(n) asm volatile("s_waitcnt vmcnt(" #n ")" ::: "memory")
#define THR(i) do { const int d_ = KNOB(i); if (d_ == 1) VMWAIT(0); else if (d_ == 2) VMWAIT(1); else if (d_ == 3) VMWAIT(2); else if (d_ == 4) VMWAIT(3); else if (d_ == 6) VMWAIT(5); else if (d_ == 8) VMWAIT(7); SB(); } while (0)
#define RBAR() do { CBAR(); __builtin_amdgcn_s_barrier(); CBAR(); } while (0)
#define LBAR() do { asm volatile("s_waitcnt lgkmcnt(0)" ::: "memory"); __builtin_amdgcn_s_barrier(); CBAR(); } while (0)
template <int N> DI float rorn(float v) { return __builtin_bit_cast(float, __builtin_amdgcn_update_dpp(0, __builtin_bit_cast(int, v), 0x120 + N, 0xf, 0xf, false)); }
DI float max3(float a, float b, float c) { float d; asm("v_max3_f32 %0, %1, %2, %3" : "=v"(d) : "v"(a), "v"(b), "v"(c)); return d; }
DI float swap16add(float x, float y) { auto t = __builtin_amdgcn_permlane16_swap(__float_as_uint(x), __float_as_uint(y), false, false); return __uint_as_float(t[0]) + __uint_as_float(t[1]); }
DI float swap32add(float x, float y) { auto t = __builtin_amdgcn_permlane32_swap(__float_as_uint(x), __float_as_uint(y), false, false); return __uint_as_float(t[0]) + __uint_as_float(t[1]); }
DI v4u selfrag(u32 vv, v4u mask) { return v4u{vv & mask.x, vv & mask.y, vv & mask.z, vv & mask.w}; }
DI u32 duplo(u32 v) { return __builtin_amdgcn_perm(v, v, 0x01000100u); }
DI u32 duphi(u32 v) { return __builtin_amdgcn_perm(v, v, 0x03020302u); }
DI int swz(int row) { return ((row & 3) << 2) | ((row >> 2) & 3); }
DI void dma(const u16* src, LAS unsigned char* dst) { __builtin_amdgcn_global_load_lds((const GAS u32*)src, (LAS u32*)dst, 16, 0, NTL); }
DI void dma_asm(const u16* src, u32 lds_addr) {
  u32 keep;
  asm volatile("s_mov_b32 %0, m0\n\ts_mov_b32 m0, %2\n\ts_nop 0\n\tglobal_load_lds_dwordx4 %1, off nt\n\ts_mov_b32 m0, %0" : "=&s"(keep) : "v"(src), "s"(lds_addr) : "memory");
}
DI v4u trfrag(LAS unsigned char* a0, LAS unsigned char* a1) {
  const v2u lo = __builtin_bit_cast(v2u, __builtin_amdgcn_ds_read_tr16_b64_v4i16((LAS s4*)a0));
  const v2u hi = __builtin_bit_cast(v2u, __builtin_amdgcn_ds_read_tr16_b64_v4i16((LAS s4*)a1));
  return v4u{lo.x, lo.y, hi.x, hi.y};
}
DI v2u mktag(u32 e1) { return v2u{e1, e1 * 0x9E3779B1u ^ 0x5bd1e995u}; }
DI u32 epoch1(rsrc_t rw) { return __builtin_amdgcn_readfirstlane(__builtin_amdgcn_raw_buffer_load_b32(rw, W_EPOCH, 0, SC1)) + 1u; }

#define ROLE_OF(b) ((b) < 128 ? ((((b) >> 3) & 2) ? 2 : 0) : (b) < 192 ? 1 : 3)
#define NROLES 4
#define STAMP(k) do {} while (0)

DI void wait_flags(rsrc_t rw, int idx, v2u tag) {
#pragma unroll 1
  for (u32 sp = 0; sp < SPIN_CAP; ++sp) {
    CBAR();
    v2u f = ldh2(rw, W_FLAG + idx * 128);
    if (!__any((f.x != tag.x) | (f.y != tag.y))) break;
    __builtin_amdgcn_s_sleep(1);
  }
  CBAR();
}

__global__ void __launch_bounds__(NT, 2) mk_kernel(Params p) {
  extern __shared__ __attribute__((aligned(16))) unsigned char smem[];
  LAS unsigned char* L = (LAS unsigned char*)smem;
  const int tid = threadIdx.x, l = tid & 63, w = __builtin_amdgcn_readfirstlane(tid >> 6);
  const int bid = blockIdx.x;
  const int r = l & 15, g = l >> 4;
  STAMP(0);
  const u64 t_start = __builtin_amdgcn_s_memrealtime();
#define HOLD(i) do { if (KNOB(i) > 0) while (__builtin_amdgcn_s_memrealtime() - t_start < (u64)KNOB(i)) __builtin_amdgcn_s_sleep(1); } while (0)
  u64 pc0; asm volatile("s_getpc_b64 %0" : "=s"(pc0));
  u32 cpf = 0;
  if (KNOB(9) == 0) { if (w >= 5) cpf = ((const GAS u32*)(pc0 & ~127ull))[umin(tid - 320, CODE_PF_BYTES / 128 - 1) * 32]; }
  else if (KNOB(9) == 1) { if (w >= 3) cpf = __builtin_nontemporal_load((const GAS u32*)(pc0 & ~127ull) + umin(tid - 192, CODE_PF_BYTES / 64 - 1) * 16); }
  else if (KNOB(9) == 2) { if (w >= 3) cpf = ((const GAS u32*)(pc0 & ~127ull))[umin(tid - 192, CODE_PF_BYTES / 64 - 1) * 16]; }
  CBAR(); SB();
  if (__builtin_expect(bid >= 192 && w < 4, 1)) {
    const u16* src = p.wuk + ((size_t)(256 * ((bid >> 3) & 1) + 64 * w + g) * H + (bid & 7) * 4 + ((bid - 192) >> 4)) * D;
#pragma unroll 1
    for (int n = 0; n < 16; ++n) dma_asm(src + n * 4 * H * D + 8 * (r ^ ((2 * n + (g >> 1)) & 15)), (u32)(size_t)L + w * 16384 + n * 1024);
  }
  SB();
  const rsrc_t rw = mk(p.ws, W_END);
  const u32 ep = __builtin_amdgcn_raw_buffer_load_b32(rw, W_EPOCH, 0, SC1);
  SB();
  const rsrc_t rx = mk(p.x, NB * M * 2);
  const u32 m16 = (l & 1) ? 0xFFFF0000u : 0x0000FFFFu;
  const int jq = (l & 7) >> 1;
  const v4u mask = {jq == 0 ? m16 : 0u, jq == 1 ? m16 : 0u, jq == 2 ? m16 : 0u, jq == 3 ? m16 : 0u};
  const int bsel = (l >> 3) & 1;
  const v4u sent4 = {SENT, SENT, SENT, SENT};
  int h, j;
  u32 e1;
#define CLEAR_BOXES(bx) do { \
    _Pragma("unroll") for (int i = 0; i < 4; ++i) sths(sent4, rw, W_OP + (bx) * 32768 + tid * 16, i * 8192); \
    sths(sent4, rw, W_PO + (bx) * 10240 + tid * 16, 0); \
    if (w < 2) sths(sent4, rw, W_PO + (bx) * 10240 + tid * 16, 8192); \
    DRAIN(); RBAR(); \
    if (tid == 0) sth2(mktag(e1), rw, W_FLAG + (64 + (bx)) * 128); } while (0)

  if (__builtin_expect(bid < 128, 1)) {
    h = (bid & 7) * 4 + (bid >> 5); j = (bid >> 3) & 3;
    const int x = h * 4 + j;
    if (__builtin_expect(w < 2, 1)) {
      const rsrc_t rq = mk(p.wq, M * H * D * 2);
      const int mb = 160 * j + 80 * w + 20 * g;
      u32 xv[10];
#pragma unroll
      for (int f = 0; f < 10; ++f) xv[f] = __builtin_amdgcn_raw_buffer_load_b32(rx, (bsel * M + mb) * 2 + 4 * f, 0, 0);
      v4u wa[20];
      const int vo = (mb * (H * D) + h * D + 8 * r) * 2;
#pragma unroll
      for (int f = 0; f < 20; ++f) { wa[f] = ldw(rq, vo, f * H * D * 2); SB(); }
      STAMP(1);
      SB();
      v4u sel[20];
#pragma unroll
      for (int f = 0; f < 10; ++f) { sel[2 * f] = selfrag(duplo(xv[f]), mask); sel[2 * f + 1] = selfrag(duphi(xv[f]), mask); }
#pragma unroll
      for (int f = 0; f < 20; ++f) PIN(sel[f]);
      e1 = __builtin_amdgcn_readfirstlane(ep) + 1u;
      int o = W_QP + (x * 2 + w) * 1024 + l * 16;
      PIN(o);
      SB();
      f4 acc = {0.f, 0.f, 0.f, 0.f};
#pragma unroll
      for (int f = 0; f < 20; ++f) acc = mfma16(sel[f], wa[f], acc);
      sth(v4u{pk2(acc.x, acc.y), e1, pk2(acc.z, acc.w), e1}, rw, o);
      STAMP(3);
    } else {
      e1 = __builtin_amdgcn_readfirstlane(ep) + 1u;
      asm volatile("" :: "v"(cpf));
    }
    RBAR();
    HOLD(6);
    if (KNOB(2) == 2 && j >= 2) CLEAR_BOXES(h * 2 + j - 2);
    if (KNOB(2) == 4 && j < 2) CLEAR_BOXES(h * 2 + j);
    if (KNOB(5) > 0 && (KNOB(1) == 0 || j < 2)) {
      if (KNOB(1) == 0) {
        const int i2 = (bid & 7) + 8 * (bid >> 4), b = (i2 & 7) >> 2, ks = (i2 & 3) + 4 * (i2 >> 3);
        const u16* src = p.ckv + ((size_t)b * SK + 128 * ks + 64 * ((bid >> 3) & 1) + 8 * w + g) * DC + 8 * r;
#pragma unroll
        for (int blk = 4 - KNOB(5); blk < 4; ++blk) { dma(src + 128 * blk, L + 131072 + w * 2048); dma(src + 128 * blk + 4 * DC, L + 131072 + w * 2048 + 1024); }
      } else {
        const int i2 = (bid & 15) + 16 * (bid >> 5), b = (i2 & 7) >> 2, ks = (i2 & 3) + 4 * (i2 >> 3);
        const u16* src = p.ckv + ((size_t)b * SK + 128 * ks + 16 * w + g) * DC + 8 * r;
#pragma unroll
        for (int blk = 4 - KNOB(5); blk < 4; ++blk)
#pragma unroll
          for (int n = 0; n < 4; ++n) dma(src + 128 * blk + 4 * n * DC, L + 131072 + w * 4096 + n * 1024);
      }
      RBAR();
    }
    if (KNOB(2) == 3 && j >= 2) CLEAR_BOXES(h * 2 + j - 2);
    STAMP(4);
  } else if (bid >= 192) {
    const int y = bid - 192;
    h = (y & 7) * 4 + (y >> 4);
    const int jj = (y >> 3) & 1;
    j = 2 + jj;
    v4u wk[16];
    if (w < 4) {
      DRAIN();
      STAMP(1);
      e1 = __builtin_amdgcn_readfirstlane(ep) + 1u;
    } else {
      __builtin_amdgcn_s_setprio(3);
      if (w == 4) sth(sent4, rw, W_QA + (((l >> 5) * H + h) * DC + 256 * jj) * 2 + (l & 31) * 16);
      DRAIN();
      e1 = __builtin_amdgcn_readfirstlane(ep) + 1u;
      if (w == 4 && l == 0) sth2(mktag(e1), rw, W_FLAG + (h * 2 + jj) * 128);
      __builtin_amdgcn_s_setprio(0);
      asm volatile("" :: "v"(cpf));
      STAMP(1);
    }
    int oa = W_QA + (((g >> 1) * H + h) * DC + 256 * jj + 64 * w + 32 * (g & 1) + 2 * r) * 2;
    int qw = 65536 + ((g >> 1) * 8 + w) * 272 + (8 * r + 4 * (g & 1)) * 2, qr = 65536 + r * 272 + 16 * g;
    int o = W_QP + (h * 8 + w) * 1024 + l * 16;
    PIN(oa); PIN(qw); PIN(qr); PIN(o);
    RBAR();
    STAMP(2);
    {
      v4u u = ldh(rw, o);
      SB();
#pragma unroll
      for (int t = 0; t < 4; ++t)
#pragma unroll
        for (int s = 0; s < 4; ++s) wk[t * 4 + s] = *(const LAS v4u*)(L + (w & 3) * 16384 + (32 * (t >> 1) + 2 * r + (t & 1)) * 256 + 16 * ((4 * s + g) ^ r));
#pragma unroll
      for (int i = 0; i < 16; ++i) PIN(wk[i]);
#pragma unroll 1
      for (u32 sp = 0; sp < SPIN_CAP; ++sp) {
        if (!__any((u.y != e1) | (u.w != e1))) break;
        CBAR();
        u = ldh(rw, o);
      }
      STAMP(3);
      *(LAS v2u*)(L + qw) = v2u{u.x, u.z};
    }
    LBAR();
    STAMP(4);
    if (w < 4) {
      v4u qf[4];
#pragma unroll
      for (int s = 0; s < 4; ++s) qf[s] = *(const LAS v4u*)(L + qr + 64 * s);
      f4 a[4];
#pragma unroll
      for (int t = 0; t < 4; ++t) a[t] = f4{0.f, 0.f, 0.f, 0.f};
#pragma unroll
      for (int s = 0; s < 4; ++s)
#pragma unroll
        for (int t = 0; t < 4; ++t) a[t] = mfma16(qf[s], wk[t * 4 + s], a[t]);
      float st[4];
#pragma unroll
      for (int t = 0; t < 4; ++t) st[t] = (a[t].x + a[t].y) + (a[t].z + a[t].w);
      sth1(guard(pk2(swap16add(st[0], st[2]), swap16add(st[1], st[3]))), rw, oa);
      STAMP(5);
    }
    RBAR();
    if (KNOB(2) == 1) CLEAR_BOXES(h * 2 + jj);
  } else {
    const int i2 = bid - 128;
    const int b = (i2 & 7) >> 2, ks = (i2 & 3) + 4 * (i2 >> 3), bk = b * 32 + ks;
    const int hh = l >> 5;
    constexpr int L_Q = 131072, L_MX = 0, L_LS = 1024, L_P = 2048, L_SL = 10240;
    if (KNOB(2) == 0) { e1 = __builtin_amdgcn_readfirstlane(ep) + 1u; CLEAR_BOXES(bk); }
    v4u fr[32];
    int sr_ = L_SL + (w & 3) * 8704 + (l >> 3) * 272 + 16 * (l & 7);
    int oo = W_OP + bk * 32768 + (l >> 3) * 1024 + (128 * (w & 3) + 8 * (l & 7)) * 2;
    if (__builtin_expect(w < 4, 1)) {
      const u16* src = p.ckv + ((size_t)b * SK + 128 * ks + 32 * w + g) * DC;
      e1 = __builtin_amdgcn_readfirstlane(ep) + 1u;
      asm volatile("" : "+s"(e1));
      HOLD(0);
      v2u fl;
#pragma unroll
      for (int n = 0; n < 32; ++n) {
        const int blk = n >> 3, r0 = 4 * (n & 7);
        dma(src + r0 * DC + 128 * blk + 8 * (r ^ ((g << 2) | (n & 3))), L + blk * 32768 + (32 * w + r0) * 256);
        if (n == 23) {
          SB();
          asm volatile("buffer_load_dwordx2 %0, %1, %2, 0 offen sc1" : "=v"(fl) : "v"(W_FLAG + (16 * w + (l >> 2)) * 128), "s"(rw) : "memory");
          SB();
        }
      }
      STAMP(1);
      const int row = 32 * w + (l & 31), sw = swz(row);
      VMWAIT(17);
      RBAR();
#pragma unroll
      for (int s = 0; s < 16; ++s) fr[s] = *(const LAS v4u*)(L + (s >> 3) * 32768 + row * 256 + 16 * ((2 * (s & 7) + hh) ^ sw));
      SB();
      asm volatile("s_waitcnt vmcnt(8)" : "+v"(fl) :: "memory");
      RBAR();
      {
        const v2u tag = mktag(e1);
        if (__any((fl.x != tag.x) | (fl.y != tag.y))) wait_flags(rw, 16 * w + (l >> 2), tag);
      }
      HOLD(11);
      v4u qv[8];
      const int qo = W_QA + b * 32768 + w * 8192 + l * 16;
#pragma unroll
      for (int i = 0; i < 8; ++i) qv[i] = ldhs(rw, qo, i * 1024);
      SB();
#pragma unroll
      for (int s = 0; s < 8; ++s) fr[16 + s] = *(const LAS v4u*)(L + 2 * 32768 + row * 256 + 16 * ((2 * s + hh) ^ sw));
      SB();
      VMWAIT(8);
      STAMP(2);
#pragma unroll
      for (int s = 0; s < 8; ++s) fr[24 + s] = *(const LAS v4u*)(L + 3 * 32768 + row * 256 + 16 * ((2 * s + hh) ^ sw));
      SB();
#pragma unroll 1
      for (u32 sp = 0; sp < SPIN_CAP; ++sp) {
        u32 mx = 0;
#pragma unroll
        for (int i = 0; i < 8; ++i) {
          mx = umax(mx, max4(qv[i]));
          *(LAS v4u*)(L + L_Q + (8 * w + i) * 1024 + 16 * (l ^ ((8 * w + i) & 15))) = qv[i];
        }
        if (!__any(mx == SENT)) break;
        CBAR();
#pragma unroll
        for (int i = 0; i < 8; ++i) qv[i] = ldhs(rw, qo, i * 1024);
      }
      STAMP(4);
      int qb = L_Q + (l & 31) * 1024;
      const int qx = (l & 15) ^ hh;
      int mxw = L_MX + ((l & 31) * 8 + w * 2 + hh) * 4;
      PIN(qb); PIN(mxw); PIN(sr_); PIN(oo);
#pragma unroll
      for (int s = 0; s < 32; ++s) PIN(fr[s]);
      STAMP(3);
      LBAR();
      STAMP(5);
      f16v sacc;
#pragma unroll
      for (int i = 0; i < 16; ++i) sacc[i] = 0.f;
      v4u qf[12];
#define QADDR(s) (L + qb + ((s) >> 3) * 256 + 16 * (((2 * (s)) & 15) ^ qx))
#pragma unroll
      for (int s = 0; s < 12; ++s) { qf[s] = *(const LAS v4u*)QADDR(s); SB(); }
#pragma unroll
      for (int s = 0; s < 32; ++s) {
        sacc = mfma32(fr[s], qf[s % 12], sacc);
        if (s + 12 < 32) qf[s % 12] = *(const LAS v4u*)QADDR(s + 12);
        SB();
      }
      float mx = fmaxf(fmaxf(sacc[0], sacc[1]), sacc[2]);
      PIN(mx); SB();
      mx = max3(max3(mx, sacc[3], sacc[4]), max3(sacc[5], sacc[6], sacc[7]), max3(max3(sacc[8], sacc[9], sacc[10]), max3(sacc[11], sacc[12], sacc[13]), max3(sacc[14], sacc[15], mx)));
      *(LAS float*)(L + mxw) = mx;
      LBAR();
      STAMP(6);
      const f4 m0 = *(const LAS f4*)(L + L_MX + (l & 31) * 32), m1 = *(const LAS f4*)(L + L_MX + (l & 31) * 32 + 16);
      mx = max3(max3(m0.x, m0.y, m0.z), max3(m0.w, m1.x, m1.y), max3(m1.z, m1.w, m1.w)) * SCL;
      float e[16];
#pragma unroll
      for (int i = 0; i < 16; ++i) e[i] = __builtin_amdgcn_exp2f(__builtin_fmaf(sacc[i], SCL, -mx));
      *(LAS v4u*)(L + L_P + ((2 * w) * 64 + l) * 16) = v4u{pk2(e[0], e[1]), pk2(e[2], e[3]), pk2(e[4], e[5]), pk2(e[6], e[7])};
      *(LAS v4u*)(L + L_P + ((2 * w + 1) * 64 + l) * 16) = v4u{pk2(e[8], e[9]), pk2(e[10], e[11]), pk2(e[12], e[13]), pk2(e[14], e[15])};
      float sum = (((e[0] + e[1]) + (e[2] + e[3])) + ((e[4] + e[5]) + (e[6] + e[7]))) + (((e[8] + e[9]) + (e[10] + e[11])) + ((e[12] + e[13]) + (e[14] + e[15])));
      *(LAS float*)(L + mxw + L_LS) = sum;
      LBAR();
      STAMP(7);
      if (w == 0 && l < 32) {
        const f4 s0 = *(const LAS f4*)(L + L_LS + l * 32), s1 = *(const LAS f4*)(L + L_LS + l * 32 + 16);
        sum = ((s0.x + s0.y) + (s0.z + s0.w)) + ((s1.x + s1.y) + (s1.z + s1.w));
        sth(v4u{__float_as_uint(mx), e1, __float_as_uint(sum), e1}, rw, W_ML + bk * 512 + l * 16);
      }
      if (KNOB(2) == 5 && w == 1 && l == 0) sth2(mktag(e1), rw, W_FLAG + (64 + bk) * 128);
      RBAR();
      v4u rb[4];
#pragma unroll
      for (int n = 0; n < 4; ++n) rb[n] = *(const LAS v4u*)(L + sr_ + n * 8 * 272);
#pragma unroll
      for (int n = 0; n < 4; ++n) sths(rb[n], rw, oo, n * 8192);
      RBAR();
    } else {
      const int w4 = w - 4;
      e1 = __builtin_amdgcn_readfirstlane(ep) + 1u;
      asm volatile("" :: "v"(cpf));
      const int bk2 = (l >> 4) & 1, q = (l & 15) >> 2, pp = l & 3;
#define VFRAGS() do { \
      _Pragma("unroll") for (int ct = 0; ct < 4; ++ct) _Pragma("unroll") for (int kk = 0; kk < 8; ++kk) { \
          const int r0 = 16 * kk + 4 * hh + q, r1 = r0 + 8, ch = 4 * ct + 2 * bk2 + (pp >> 1); \
          LAS unsigned char* base = L + w4 * 32768 + 8 * (pp & 1); \
          fr[ct * 8 + kk] = trfrag(base + r0 * 256 + 16 * (ch ^ swz(r0)), base + r1 * 256 + 16 * (ch ^ swz(r1))); } \
      _Pragma("unroll") for (int s = 0; s < 32; ++s) PIN(fr[s]); } while (0)
      const v2u tag = mktag(e1);
      v2u fl = tag;
      const int nb = w4 < 2 ? 1 : w4;
#pragma unroll 1
      for (int k = 0; k < nb; ++k) RBAR();
      if (w4 == 3) { STAMP(5); if (KNOB(2) != 5) fl = ldh2(rw, W_FLAG + (64 + bk) * 128); }
      SB();
      VFRAGS();
      if (w4 == 3 && __any((fl.x != tag.x) | (fl.y != tag.y))) wait_flags(rw, 64 + bk, tag);
      STAMP(3);
      asm volatile("s_waitcnt lgkmcnt(0)" ::: "memory");
#pragma unroll 1
      for (int k = nb; k < 3; ++k) RBAR();
      int sw_ = L_SL + w4 * 8704 + (l & 31) * 272 + 8 * hh;
      PIN(sw_); PIN(sr_); PIN(oo);
      if (KNOB(2) == 5) {
        if (w4 < 3) {
#pragma unroll
          for (int i = 0; i < 14; ++i) {
            const int u = w4 * 14 + i;
            sths(sent4, rw, l * 16, u < 32 ? W_OP + bk * 32768 + u * 1024 : W_PO + bk * 10240 + (u - 32) * 1024);
          }
        }
        STAMP(1);
      }
      RBAR();
      if (KNOB(2) == 5) { DRAIN(); STAMP(2); }
      RBAR();
      STAMP(7);
      v4u pf[8];
#pragma unroll
      for (int kk = 0; kk < 8; ++kk) pf[kk] = *(const LAS v4u*)(L + L_P + (kk * 64 + l) * 16);
      SB();
      f16v o[4];
#pragma unroll
      for (int ct = 0; ct < 5; ++ct) {
        if (ct < 4) {
#pragma unroll
          for (int i = 0; i < 16; ++i) o[ct][i] = 0.f;
        }
#pragma unroll
        for (int kk = 0; kk < 8; ++kk) {
          if (ct < 4) o[ct] = mfma32(fr[ct * 8 + kk], pf[kk], o[ct]);
          SB();
          if (ct > 0 && kk >= 1 && kk < 5) {
            const int g4 = kk - 1, pt = ct - 1;
            *(LAS v2u*)(L + sw_ + (32 * pt + 8 * g4) * 2) = v2u{guard(pk2(o[pt][4 * g4], o[pt][4 * g4 + 1])), guard(pk2(o[pt][4 * g4 + 2], o[pt][4 * g4 + 3]))};
          }
          if (ct == 2 && kk == 6) LBAR();
          SB();
        }
      }
      STAMP(6);
      LBAR();
    }
    {
      const int half = (w >> 2) * 2;
      const v4u r0 = *(const LAS v4u*)(L + sr_ + half * 8 * 272 + 128), r1 = *(const LAS v4u*)(L + sr_ + (half + 1) * 8 * 272 + 128);
      sth(r0, rw, oo + half * 8192 + 128);
      sth(r1, rw, oo + half * 8192 + 8192 + 128);
    }
    STAMP(8);
    if (i2 < 40) {
      const int du = i2, db = du / 20;
      float gate;
      {
        const rsrc_t rg = mk(p.gw, M * 2);
        float s = 0.f;
#pragma unroll
        for (int i = 0; i < 5; ++i) {
          const u32 xa = __builtin_amdgcn_raw_buffer_load_b32(rx, (db * M + 128 * i + 2 * l) * 2, 0, 0);
          const u32 ga = __builtin_amdgcn_raw_buffer_load_b32(rg, (128 * i + 2 * l) * 2, 0, 0);
          s += bflo(xa) * bflo(ga) + bfhi(xa) * bfhi(ga);
        }
        s += rorn<1>(s); s += rorn<2>(s); s += rorn<4>(s); s += rorn<8>(s);
        s = swap16add(s, s); s = swap32add(s, s);
        gate = 1.f / (1.f + __expf(-s));
      }
      const int pr = tid >> 3;
      wait_flags(rw, 64 + 4 * w + ((l >> 3) & 3) + 32 * (l >> 5), mktag(e1));
      int lw = ((4 * (l & 7) + g) * 8 + w) * 4;
      PIN(lw);
      LAS unsigned char* LD = L + 65536;
      STAMP(9);
      f4 v;
      {
        v4u t0, t1, f0, f1;
        const int o = W_PO + pr * 5120 + du * 128 + (l & 7) * 16;
        const int of = W_PF + (int)umin(pr, 53) * 10240 + du * 256 + (l & 7) * 32;
        bool fok = false;
#pragma unroll 1
        for (u32 sp = 0; sp < SPIN_CAP; ++sp) {
          CBAR();
          t0 = ldh(rw, o); t1 = ldhs(rw, o, 64 * 5120);
          if (!fok) {
            f0 = ldh(rw, of); f1 = ldh(rw, of + 16);
            fok = !__any((f0.y != e1) | (f0.w != e1) | (f1.y != e1) | (f1.w != e1));
          }
          if (fok && !__any(umax(max4(t0), max4(t1)) == SENT)) break;
        }
        const float gf = pr < 54 ? 1.f - gate : 0.f;
        v = f4{__uint_as_float(f0.x), __uint_as_float(f0.z), __uint_as_float(f1.x), __uint_as_float(f1.z)} * gf + (__builtin_bit_cast(f4, t0) + __builtin_bit_cast(f4, t1)) * gate;
      }
      STAMP(10);
      float s = swap32add(swap16add(v.x, v.y), swap16add(v.z, v.w));
      s += rorn<8>(s);
      *(LAS float*)(LD + lw) = s;
      LBAR();
      if (w == 0) {
        const f4 s0 = *(const LAS f4*)(LD + (l & 31) * 32), s1 = *(const LAS f4*)(LD + (l & 31) * 32 + 16);
        const float t = ((s0.x + s0.y) + (s0.z + s0.w)) + ((s1.x + s1.y) + (s1.z + s1.w));
        if (l < 32) p.out[32 * du + l] = (u16)pk2(t, t);
        if (du == 0 && l == 0) sth1(e1, rw, W_EPOCH);
      }
      STAMP(11);
    }
    goto done;
  }
  if (bid >= 128 || j < 2) {
    const int ac = h * 4 + j;
    HOLD(3);
    const int cj = j >> 1, dj = j & 1;
    {
      const u16* sv = p.wuv + ((size_t)(256 * cj + 32 * w + (l >> 3)) * H + h) * D + 64 * dj;
#pragma unroll
      for (int n = 0; n < 4; ++n) {
        const int fz = 2 * (((l >> 4) & 1) | ((n & 1) << 1));
        if (n < KNOB(10) && bid < 128) THR(7);
        dma(sv + n * 8 * H * D + 8 * ((l & 7) ^ fz), L + (w * 4 + n) * 1024);
      }
      const u16* so = p.wo + ((size_t)h * D + 64 * dj + 8 * w + g) * M;
      const int fo = 2 * (g | ((w & 1) << 2));
#pragma unroll
      for (int n = 0; n < 10; ++n) { if (n + 4 < KNOB(10) && bid < 128) THR(7); dma(so + (n & 1) * 4 * M + (n >> 1) * 128 + 8 * (r ^ fo), L + 32768 + (n >> 1) * 16384 + (8 * w + 4 * (n & 1)) * 256); }
    }
    const int cb_b = w >> 2, cb = (w >> 1) & 1, kh = w & 1;
    constexpr int L_A2 = 114688, L_A3 = L_A2 + 4 * 528, L_WT = 118784;
    int a2w = L_A2 + (cb_b * 2 + kh) * 528 + (128 * cb + 8 * r + 4 * (g & 1)) * 2;
    int a2r = L_A2 + (r & 3) * 528 + 16 * g;
    int a3w = L_A3 + (g & 1) * 144 + (16 * (w & 3) + r) * 2;
    int a3r = L_A3 + (r >> 3) * 144 + 16 * g;
    int po = W_PO + ac * 5120 + ((g >> 1) * M + 32 * w + 16 * (g & 1) + r) * 4;
    int oo = W_OP + (((cb_b * 32 + 16 * kh + g) * H + h) * DC + 256 * cj + 128 * cb + 8 * r) * 2;
    PIN(a2w); PIN(a2r); PIN(a3w); PIN(a3r); PIN(po); PIN(oo);
    if (KNOB(2) != 5) wait_flags(rw, 64 + 8 * w + (l >> 3), mktag(e1));
    DRAIN();
    RBAR();
    STAMP(6);
    v4u of[4], ml = {0u, 0u, 0u, 0u};
    if (w == 7) ml = ldh(rw, W_ML + (l * H + h) * 16);
#pragma unroll
    for (int i = 0; i < 4; ++i) of[i] = KNOB(2) == 5 ? sent4 : ldhs(rw, oo, i * 4 * H * DC * 2);
    SB();
    v4u wv[8], wo[12];
    {
      const int q = r >> 2, pp = l & 3;
#pragma unroll
      for (int ks = 0; ks < 8; ++ks) {
        const int row = 32 * ks + 8 * g + q, fz = 2 * (((q >> 1) & 1) | ((g & 1) << 1));
        LAS unsigned char* a = L + row * 128 + 16 * ((2 * (w & 3) + (pp >> 1)) ^ fz) + 8 * (pp & 1);
        wv[ks] = trfrag(a, a + 512);
      }
#pragma unroll
      for (int pi = 0; pi < 3; ++pi) {
        const int P = (pi == 2 ? (w & 3) : w) + 8 * pi;
#pragma unroll
        for (int tt = 0; tt < 2; ++tt)
#pragma unroll
          for (int ks = 0; ks < 2; ++ks) {
            const int row = 32 * ks + 8 * g + q, fo = 2 * (q | ((g & 1) << 2));
            LAS unsigned char* a = L + 32768 + (P >> 2) * 16384 + row * 256 + 16 * ((4 * (P & 3) + 2 * tt + (pp >> 1)) ^ fo) + 8 * (pp & 1);
            wo[pi * 4 + tt * 2 + ks] = trfrag(a, a + 1024);
          }
      }
#pragma unroll
      for (int i = 0; i < 8; ++i) PIN(wv[i]);
#pragma unroll
      for (int i = 0; i < 12; ++i) PIN(wo[i]);
    }
    STAMP(11);
    if (w == 7) {
#pragma unroll 1
      for (u32 sp = 0; sp < SPIN_CAP; ++sp) {
        if (!__any((ml.y != e1) | (ml.w != e1))) break;
        CBAR();
        ml = ldh(rw, W_ML + (l * H + h) * 16);
      }
      STAMP(10);
      const float mm = __uint_as_float(ml.x), ll = __uint_as_float(ml.z);
      float mx = mm;
      mx = fmaxf(mx, rorn<1>(mx)); mx = fmaxf(mx, rorn<2>(mx)); mx = fmaxf(mx, rorn<4>(mx)); mx = fmaxf(mx, rorn<8>(mx));
      { auto t = __builtin_amdgcn_permlane16_swap(__float_as_uint(mx), __float_as_uint(mx), false, false); mx = fmaxf(__uint_as_float(t[0]), __uint_as_float(t[1])); }
      const float e = __builtin_amdgcn_exp2f(mm - mx);
      float sum = e * ll;
      sum += rorn<1>(sum); sum += rorn<2>(sum); sum += rorn<4>(sum); sum += rorn<8>(sum);
      sum = swap16add(sum, sum);
      const float wgt = e * __builtin_amdgcn_rcpf(sum);
      *(LAS u32*)(L + L_WT + ((l & 48) + (l & 3) * 4 + ((l >> 2) & 3)) * 4) = pk2(wgt, wgt);
    }
    LBAR();
    STAMP(7);
    v4u wsel[4];
    {
      const v4u wt = *(const LAS v4u*)(L + L_WT + (cb_b * 32 + kh * 16 + g * 4) * 4);
      bool bad = __any(umax(umax(max4(of[0]), max4(of[1])), umax(max4(of[2]), max4(of[3]))) == SENT);
      if (bad) {
#pragma unroll
        for (int i = 0; i < 4; ++i) of[i] = ldhs(rw, oo, i * 4 * H * DC * 2);
      }
      SB();
      wsel[0] = selfrag(wt.x, mask); wsel[1] = selfrag(wt.y, mask); wsel[2] = selfrag(wt.z, mask); wsel[3] = selfrag(wt.w, mask);
#pragma unroll
      for (int i = 0; i < 4; ++i) PIN(wsel[i]);
      SB();
#pragma unroll 1
      for (u32 sp = 0; sp < SPIN_CAP; ++sp) {
        u32 bd = 0;
#pragma unroll
        for (int i = 0; i < 4; ++i) bd |= __any(max4(of[i]) == SENT) ? 1u << i : 0u;
        if (!bd) break;
        CBAR(); SB();
#pragma unroll
        for (int i = 0; i < 4; ++i) if (bd >> i & 1) of[i] = ldhs(rw, oo, i * 4 * H * DC * 2);
      }
    }
    STAMP(8);
    {
      f4 acc = {0.f, 0.f, 0.f, 0.f};
#pragma unroll
      for (int i = 0; i < 4; ++i) acc = mfma16(wsel[i], of[i], acc);
      *(LAS v2u*)(L + a2w) = v2u{pk2(acc.x, acc.y), pk2(acc.z, acc.w)};
    }
    LBAR();
    if (w < 4) {
      v4u af[8];
#pragma unroll
      for (int ks = 0; ks < 8; ++ks) af[ks] = *(const LAS v4u*)(L + a2r + 64 * ks);
      SB();
      f4 acc = {0.f, 0.f, 0.f, 0.f};
#pragma unroll
      for (int ks = 0; ks < 8; ++ks) acc = mfma16(af[ks], wv[ks], acc);
      const float v = (g & 1) ? acc.z + acc.w : acc.x + acc.y;
      *(LAS u16*)(L + a3w) = (u16)pk2(v, v);
    }
    LBAR();
    {
      const v4u a0 = *(const LAS v4u*)(L + a3r), a1 = *(const LAS v4u*)(L + a3r + 64);
#pragma unroll
      for (int pi = 0; pi < 3; ++pi) {
        if (pi == 2 && w >= 4) break;
        f4 t0 = {0.f, 0.f, 0.f, 0.f}, t1 = {0.f, 0.f, 0.f, 0.f};
        t0 = mfma16(a0, wo[pi * 4 + 0], t0); t1 = mfma16(a0, wo[pi * 4 + 2], t1);
        t0 = mfma16(a1, wo[pi * 4 + 1], t0); t1 = mfma16(a1, wo[pi * 4 + 3], t1);
        sth1(guard(__float_as_uint((g & 1) ? t1.x : t0.x)), rw, po + pi * 1024);
      }
    }
    STAMP(9);
  } else {
    const int i3 = h * 2 + j - 2;
    HOLD(4);
    if (i3 < 54) {
      const int pair = i3 >> 1, half = i3 & 1;
      STAMP(6);
      u32 xv[5];
#pragma unroll
      for (int f = 0; f < 5; ++f) xv[f] = __builtin_amdgcn_raw_buffer_load_b32(rx, (bsel * M + 320 * half + 40 * w + 10 * g) * 2 + 4 * f, 0, 0);
      v4u wf[10], wdn[8];
      const GAS u16* src = (const GAS u16*)((r >> 3) ? p.wu : p.wg) + (320 * half + 40 * w + 10 * g) * F + 64 * pair + 8 * (r & 7);
      if (w >= 1 && w < 6) {
        const rsrc_t rd = mk(p.wd, F * M * 2);
        const int vo = ((32 * i3 + 8 * g) * M + 128 * (w - 1) + 8 * r) * 2;
#pragma unroll
        for (int f = 0; f < 8; ++f) { if (f < KNOB(10)) THR(8); wdn[f] = ldw(rd, vo, f * M * 2); }
      } else {
#pragma unroll
        for (int f = 0; f < 8; ++f) wdn[f] = v4u{0u, 0u, 0u, 0u};
      }
      SB();
#pragma unroll
      for (int f = 0; f < 10; ++f) { if (f < KNOB(10) && (w == 0 || w >= 6)) THR(8); wf[f] = __builtin_nontemporal_load((const GAS v4u*)(src + f * F)); SB(); }
      f4 acc = {0.f, 0.f, 0.f, 0.f};
#pragma unroll
      for (int f = 0; f < 5; ++f) {
        acc = mfma16(selfrag(duplo(xv[f]), mask), wf[2 * f], acc);
        acc = mfma16(selfrag(duphi(xv[f]), mask), wf[2 * f + 1], acc);
      }
      *(LAS f4*)(L + (w * 64 + l) * 16) = acc;
      STAMP(7);
      LBAR();
      if (w == 0) {
        f4 s = *(const LAS f4*)(L + l * 16);
#pragma unroll
        for (int i = 1; i < 8; ++i) s += *(const LAS f4*)(L + (i * 64 + l) * 16);
        sth(v4u{__float_as_uint(s.x), e1, __float_as_uint(s.y), e1}, rw, W_FX + i3 * 2048 + l * 16);
        sth(v4u{__float_as_uint(s.z), e1, __float_as_uint(s.w), e1}, rw, W_FX + i3 * 2048 + 1024 + l * 16);
        v4u z0, z1;
#pragma unroll 1
        for (u32 sp = 0; sp < SPIN_CAP; ++sp) {
          CBAR();
          z0 = ldh(rw, W_FX + (i3 ^ 1) * 2048 + l * 16); z1 = ldh(rw, W_FX + (i3 ^ 1) * 2048 + 1024 + l * 16);
          if (!__any((z0.y != e1) | (z0.w != e1) | (z1.y != e1) | (z1.w != e1))) break;
        }
        const f4 o = {__uint_as_float(z0.x), __uint_as_float(z0.z), __uint_as_float(z1.x), __uint_as_float(z1.z)};
        s += o;
        float hv[4];
#pragma unroll
        for (int i = 0; i < 4; ++i) { const float gs = s[i], us = rorn<8>(s[i]); hv[i] = gs * __builtin_amdgcn_rcpf(1.f + __expf(-gs)) * us; }
        if ((r >> 2) == half) *(LAS v2u*)(L + 8192 + (g >> 1) * 64 + (8 * (r & 3) + 4 * (g & 1)) * 2) = v2u{pk2(hv[0], hv[1]), pk2(hv[2], hv[3])};
      }
      LBAR();
      if (w >= 1 && w < 6) {
        const v4u z = *(const LAS v4u*)(L + 8192 + bsel * 64 + g * 16);
        const u32 zz[4] = {z.x, z.y, z.z, z.w};
        acc = f4{0.f, 0.f, 0.f, 0.f};
#pragma unroll
        for (int f = 0; f < 4; ++f) {
          acc = mfma16(selfrag(duplo(zz[f]), mask), wdn[2 * f], acc);
          acc = mfma16(selfrag(duphi(zz[f]), mask), wdn[2 * f + 1], acc);
        }
        const int o = W_PF + i3 * 10240 + ((g >> 1) * M + 128 * (w - 1) + 8 * r + 4 * (g & 1)) * 8;
        sth(v4u{__float_as_uint(acc.x), e1, __float_as_uint(acc.y), e1}, rw, o);
        sth(v4u{__float_as_uint(acc.z), e1, __float_as_uint(acc.w), e1}, rw, o + 16);
      }
      STAMP(8);
    }
  }
done:;
}

extern "C" void kernel_launch(void* const* d_in, const int* in_sizes, int n_in,
                              void* d_out, int out_size, void* d_ws, size_t ws_size,
                              hipStream_t stream) {
  Params p;
  p.x = (const u16*)d_in[0]; p.ckv = (const u16*)d_in[1]; p.wq = (const u16*)d_in[2]; p.wuk = (const u16*)d_in[3];
  p.wuv = (const u16*)d_in[4]; p.wo = (const u16*)d_in[5]; p.wg = (const u16*)d_in[7]; p.wu = (const u16*)d_in[8];
  p.wd = (const u16*)d_in[9]; p.gw = (const u16*)d_in[10]; p.out = (u16*)d_out; p.ws = (unsigned char*)d_ws + WS_OFF(ws_size);
  (void)hipFuncSetAttribute((const void*)mk_kernel, hipFuncAttributeMaxDynamicSharedMemorySize, LDS_BYTES);
  mk_kernel<<<256, NT, LDS_BYTES, stream>>>(p);
}
```

```cpp
#include <hip/hip_runtime.h>
#include <stdint.h>

#define STAMPS 0
#define WS_OFF(n) 0
#define TUNE 0
#define KNOB(i) kKnob[i]
#define KSET 0, 0, 5, 0, 0, 2, 0, 0, 0, 0, 0, 0
constexpr int kKnob[12] = {KSET};

typedef unsigned short u16;
typedef unsigned int u32;
typedef unsigned long long u64;
typedef u32 v4u __attribute__((ext_vector_type(4)));
typedef u32 v2u __attribute__((ext_vector_type(2)));
typedef float f4 __attribute__((ext_vector_type(4)));
typedef float f2 __attribute__((ext_vector_type(2)));
typedef float f16v __attribute__((ext_vector_type(16)));
typedef short bf8 __attribute__((ext_vector_type(8)));
typedef short s4 __attribute__((ext_vector_type(4)));
typedef __bf16 bf2 __attribute__((ext_vector_type(2)));
#define GAS __attribute__((address_space(1)))
#define LAS __attribute__((address_space(3)))
#define DI __device__ __forceinline__
typedef __amdgpu_buffer_rsrc_t rsrc_t;

constexpr int NB = 2, H = 32, SK = 4096, D = 128, M = 640, DC = 512, F = 1728;
constexpr int NT = 512;
constexpr int W_EPOCH = 0;
constexpr int W_FLAG = 4096;
constexpr int W_QA = 65536;
constexpr int W_ML = 24576;
constexpr int W_FX = 131072;
constexpr int W_QP = 262144;
constexpr int W_PO = 524288;
constexpr int W_PF = 1179648;
constexpr int W_OP = 2097152;
constexpr int W_END = 4194304;
constexpr int W_DBG = 8388608;
constexpr int LDS_BYTES = 163840;
constexpr int SC1 = 16, NTL = 2;
constexpr u32 SENT = 0xFFFFFFFFu;
constexpr u32 SPIN_CAP = 1u << 20;
#define CODE_PF 16896
constexpr int CODE_PF_BYTES = CODE_PF;
constexpr float SCL = 0.08838834764831845f * 1.4426950408889634f;

struct Params {
  const u16* x; const u16* ckv; const u16* wq; const u16* wuk; const u16* wuv; const u16* wo;
  const u16* wg; const u16* wu; const u16* wd; const u16* gw; u16* out; unsigned char* ws;
};

DI rsrc_t mk(const void* p, int bytes) { return __builtin_amdgcn_make_buffer_rsrc((void*)p, 0, bytes, 0x00020000); }
DI v4u ldw(rsrc_t r, int voff, int soff) { return __builtin_amdgcn_raw_buffer_load_b128(r, voff, soff, NTL); }
DI v4u ldh(rsrc_t r, int voff) { return __builtin_amdgcn_raw_buffer_load_b128(r, voff, 0, SC1); }
DI v4u ldhs(rsrc_t r, int voff, int soff) { return __builtin_amdgcn_raw_buffer_load_b128(r, voff, soff, SC1); }
DI v2u ldh2(rsrc_t r, int voff) { return __builtin_amdgcn_raw_buffer_load_b64(r, voff, 0, SC1); }
DI void sth(v4u v, rsrc_t r, int voff) { __builtin_amdgcn_raw_buffer_store_b128(v, r, voff, 0, SC1); }
DI void sths(v4u v, rsrc_t r, int voff, int soff) { __builtin_amdgcn_raw_buffer_store_b128(v, r, voff, soff, SC1); }
DI void sth2(v2u v, rsrc_t r, int voff) { __builtin_amdgcn_raw_buffer_store_b64(v, r, voff, 0, SC1); }
DI void sth1(u32 v, rsrc_t r, int voff) { __builtin_amdgcn_raw_buffer_store_b32(v, r, voff, 0, SC1); }
DI u32 umin(u32 a, u32 b) { return a < b ? a : b; }
DI u32 umax(u32 a, u32 b) { return a > b ? a : b; }
DI u32 guard(u32 v) { return umin(v, 0xFFFFFFFEu); }
DI v4u guard4(v4u v) { return v4u{guard(v.x), guard(v.y), guard(v.z), guard(v.w)}; }
DI u32 max4(v4u v) { return umax(umax(v.x, v.y), umax(v.z, v.w)); }
DI u32 pk2(float lo, float hi) { bf2 r = {(__bf16)lo, (__bf16)hi}; return __builtin_bit_cast(u32, r); }
DI float bflo(u32 v) { return __uint_as_float(v << 16); }
DI float bfhi(u32 v) { return __uint_as_float(v & 0xFFFF0000u); }
DI f4 mfma16(v4u a, v4u b, f4 c) { return __builtin_amdgcn_mfma_f32_16x16x32_bf16(__builtin_bit_cast(bf8, a), __builtin_bit_cast(bf8, b), c, 0, 0, 0); }
DI f16v mfma32(v4u a, v4u b, f16v c) { return __builtin_amdgcn_mfma_f32_32x32x16_bf16(__builtin_bit_cast(bf8, a), __builtin_bit_cast(bf8, b), c, 0, 0, 0); }
#define CBAR() asm volatile("" ::: "memory")
#define SB() __builtin_amdgcn_sched_barrier(0)
#define PIN(x) asm volatile("" : "+v"(x))
#define DRAIN() asm volatile("s_waitcnt vmcnt(0)" ::: "memory")
#define VMWAIT(n) asm volatile("s_waitcnt vmcnt(" #n ")" ::: "memory")
#define THR(i) do { const int d_ = KNOB(i); if (d_ == 1) VMWAIT(0); else if (d_ == 2) VMWAIT(1); else if (d_ == 3) VMWAIT(2); else if (d_ == 4) VMWAIT(3); else if (d_ == 6) VMWAIT(5); else if (d_ == 8) VMWAIT(7); SB(); } while (0)
#define RBAR() do { CBAR(); __builtin_amdgcn_s_barrier(); CBAR(); } while (0)
#define LBAR() do { asm volatile("s_waitcnt lgkmcnt(0)" ::: "memory"); __builtin_amdgcn_s_barrier(); CBAR(); } while (0)
template <int N> DI float rorn(float v) { return __builtin_bit_cast(float, __builtin_amdgcn_update_dpp(0, __builtin_bit_cast(int, v), 0x120 + N, 0xf, 0xf, false)); }
DI float max3(float a, float b, float c) { float d; asm("v_max3_f32 %0, %1, %2, %3" : "=v"(d) : "v"(a), "v"(b), "v"(c)); return d; }
DI float swap16add(float x, float y) { auto t = __builtin_amdgcn_permlane16_swap(__float_as_uint(x), __float_as_uint(y), false, false); return __uint_as_float(t[0]) + __uint_as_float(t[1]); }
DI float swap32add(float x, float y) { auto t = __builtin_amdgcn_permlane32_swap(__float_as_uint(x), __float_as_uint(y), false, false); return __uint_as_float(t[0]) + __uint_as_float(t[1]); }
DI v4u selfrag(u32 vv, v4u mask) { return v4u{vv & mask.x, vv & mask.y, vv & mask.z, vv & mask.w}; }
DI u32 duplo(u32 v) { return __builtin_amdgcn_perm(v, v, 0x01000100u); }
DI u32 duphi(u32 v) { return __builtin_amdgcn_perm(v, v, 0x03020302u); }
DI int swz(int row) { return ((row & 3) << 2) | ((row >> 2) & 3); }
DI void dma(const u16* src, LAS unsigned char* dst) { __builtin_amdgcn_global_load_lds((const GAS u32*)src, (LAS u32*)dst, 16, 0, NTL); }
DI void dma_asm(const u16* src, u32 lds_addr) {
  u32 keep;
  asm volatile("s_mov_b32 %0, m0\n\ts_mov_b32 m0, %2\n\ts_nop 0\n\tglobal_load_lds_dwordx4 %1, off nt\n\ts_mov_b32 m0, %0" : "=&s"(keep) : "v"(src), "s"(lds_addr) : "memory");
}
DI v4u trfrag(LAS unsigned char* a0, LAS unsigned char* a1) {
  const v2u lo = __builtin_bit_cast(v2u, __builtin_amdgcn_ds_read_tr16_b64_v4i16((LAS s4*)a0));
  const v2u hi = __builtin_bit_cast(v2u, __builtin_amdgcn_ds_read_tr16_b64_v4i16((LAS s4*)a1));
  return v4u{lo.x, lo.y, hi.x, hi.y};
}
DI v2u mktag(u32 e1) { return v2u{e1, e1 * 0x9E3779B1u ^ 0x5bd1e995u}; }
DI u32 epoch1(rsrc_t rw) { return __builtin_amdgcn_readfirstlane(__builtin_amdgcn_raw_buffer_load_b32(rw, W_EPOCH, 0, SC1)) + 1u; }

#define ROLE_OF(b) ((b) < 128 ? ((((b) >> 3) & 2) ? 2 : 0) : (b) < 192 ? 1 : 3)
#define NROLES 4
#define STAMP(k) do {} while (0)

DI void wait_flags(rsrc_t rw, int idx, v2u tag) {
#pragma unroll 1
  for (u32 sp = 0; sp < SPIN_CAP; ++sp) {
    CBAR();
    v2u f = ldh2(rw, W_FLAG + idx * 128);
    if (!__any((f.x != tag.x) | (f.y != tag.y))) break;
    __builtin_amdgcn_s_sleep(1);
  }
  CBAR();
}

__global__ void __launch_bounds__(NT, 2) mk_kernel(Params p) {
  extern __shared__ __attribute__((aligned(16))) unsigned char smem[];
  LAS unsigned char* L = (LAS unsigned char*)smem;
  const int tid = threadIdx.x, l = tid & 63, w = __builtin_amdgcn_readfirstlane(tid >> 6);
  const int bid = blockIdx.x;
  const int r = l & 15, g = l >> 4;
  STAMP(0);
  const u64 t_start = __builtin_amdgcn_s_memrealtime();
#define HOLD(i) do { if (KNOB(i) > 0) while (__builtin_amdgcn_s_memrealtime() - t_start < (u64)KNOB(i)) __builtin_amdgcn_s_sleep(1); } while (0)
  u64 pc0; asm volatile("s_getpc_b64 %0" : "=s"(pc0));
  u32 cpf = 0;
  if (KNOB(9) == 0) { if (w >= 5) cpf = ((const GAS u32*)(pc0 & ~127ull))[umin(tid - 320, CODE_PF_BYTES / 128 - 1) * 32]; }
  else if (KNOB(9) == 1) { if (w >= 3) cpf = __builtin_nontemporal_load((const GAS u32*)(pc0 & ~127ull) + umin(tid - 192, CODE_PF_BYTES / 64 - 1) * 16); }
  else if (KNOB(9) == 2) { if (w >= 3) cpf = ((const GAS u32*)(pc0 & ~127ull))[umin(tid - 192, CODE_PF_BYTES / 64 - 1) * 16]; }
  CBAR(); SB();
  if (__builtin_expect(bid >= 192 && w < 4, 1)) {
    const u16* src = p.wuk + ((size_t)(64 * ((bid - 192) >> 3) + 16 * w) * H + (bid & 7) * 4 + g) * D;
#pragma unroll 1
    for (int n = 0; n < 16; ++n) dma_asm(src + n * H * D + 8 * (r ^ ((8 * w + (n >> 1)) & 15)), (u32)(size_t)L + w * 16384 + n * 1024);
  }
  SB();
  const rsrc_t rw = mk(p.ws, W_END);
  const u32 ep = __builtin_amdgcn_raw_buffer_load_b32(rw, W_EPOCH, 0, SC1);
  SB();
  const rsrc_t rx = mk(p.x, NB * M * 2);
  const u32 m16 = (l & 1) ? 0xFFFF0000u : 0x0000FFFFu;
  const int jq = (l & 7) >> 1;
  const v4u mask = {jq == 0 ? m16 : 0u, jq == 1 ? m16 : 0u, jq == 2 ? m16 : 0u, jq == 3 ? m16 : 0u};
  const int bsel = (l >> 3) & 1;
  const v4u sent4 = {SENT, SENT, SENT, SENT};
  int h, j;
  u32 e1;
#define CLEAR_BOXES(bx) do { \
    _Pragma("unroll") for (int i = 0; i < 4; ++i) sths(sent4, rw, W_OP + (bx) * 32768 + tid * 16, i * 8192); \
    sths(sent4, rw, W_PO + (bx) * 10240 + tid * 16, 0); \
    if (w < 2) sths(sent4, rw, W_PO + (bx) * 10240 + tid * 16, 8192); \
    DRAIN(); RBAR(); \
    if (tid == 0) sth2(mktag(e1), rw, W_FLAG + (64 + (bx)) * 128); } while (0)

  if (__builtin_expect(bid < 128, 1)) {
    h = (bid & 7) * 4 + (bid >> 5); j = (bid >> 3) & 3;
    const int x = h * 4 + j;
    if (__builtin_expect(w < 2, 1)) {
      const rsrc_t rq = mk(p.wq, M * H * D * 2);
      const int mb = 160 * j + 80 * w + 20 * g;
      u32 xv[10];
#pragma unroll
      for (int f = 0; f < 10; ++f) xv[f] = __builtin_amdgcn_raw_buffer_load_b32(rx, (bsel * M + mb) * 2 + 4 * f, 0, 0);
      v4u wa[20];
      const int vo = (mb * (H * D) + h * D + 8 * r) * 2;
#pragma unroll
      for (int f = 0; f < 20; ++f) { wa[f] = ldw(rq, vo, f * H * D * 2); SB(); }
      STAMP(1);
      SB();
      v4u sel[20];
#pragma unroll
      for (int f = 0; f < 10; ++f) { sel[2 * f] = selfrag(duplo(xv[f]), mask); sel[2 * f + 1] = selfrag(duphi(xv[f]), mask); }
#pragma unroll
      for (int f = 0; f < 20; ++f) PIN(sel[f]);
      e1 = __builtin_amdgcn_readfirstlane(ep) + 1u;
      int o = W_QP + (x * 2 + w) * 1024 + l * 16;
      PIN(o);
      SB();
      f4 acc = {0.f, 0.f, 0.f, 0.f};
#pragma unroll
      for (int f = 0; f < 20; ++f) acc = mfma16(sel[f], wa[f], acc);
      sth(v4u{pk2(acc.x, acc.y), e1, pk2(acc.z, acc.w), e1}, rw, o);
      STAMP(3);
    } else {
      e1 = __builtin_amdgcn_readfirstlane(ep) + 1u;
      asm volatile("" :: "v"(cpf));
    }
    RBAR();
    HOLD(6);
    if (KNOB(2) == 2 && j >= 2) CLEAR_BOXES(h * 2 + j - 2);
    if (KNOB(2) == 4 && j < 2) CLEAR_BOXES(h * 2 + j);
    if (KNOB(5) > 0 && (KNOB(1) == 0 || j < 2)) {
      if (KNOB(1) == 0) {
        const int i2 = (bid & 7) + 8 * (bid >> 4), b = (i2 & 7) >> 2, ks = (i2 & 3) + 4 * (i2 >> 3);
        const u16* src = p.ckv + ((size_t)b * SK + 128 * ks + 64 * ((bid >> 3) & 1) + 8 * w + g) * DC + 8 * r;
#pragma unroll
        for (int blk = 4 - KNOB(5); blk < 4; ++blk) { dma(src + 128 * blk, L + 131072 + w * 2048); dma(src + 128 * blk + 4 * DC, L + 131072 + w * 2048 + 1024); }
      } else {
        const int i2 = (bid & 15) + 16 * (bid >> 5), b = (i2 & 7) >> 2, ks = (i2 & 3) + 4 * (i2 >> 3);
        const u16* src = p.ckv + ((size_t)b * SK + 128 * ks + 16 * w + g) * DC + 8 * r;
#pragma unroll
        for (int blk = 4 - KNOB(5); blk < 4; ++blk)
#pragma unroll
          for (int n = 0; n < 4; ++n) dma(src + 128 * blk + 4 * n * DC, L + 131072 + w * 4096 + n * 1024);
      }
      RBAR();
    }
    if (KNOB(2) == 3 && j >= 2) CLEAR_BOXES(h * 2 + j - 2);
    STAMP(4);
  } else if (bid >= 192) {
    const int y = bid - 192;
    h = (y & 7) * 4 + (y >> 4);
    const int jj = (y >> 3) & 1;
    j = 2 + jj;
    const int hg4 = (y & 7) * 4, cs = y >> 3;
    v4u wk[16];
    if (w < 4) {
      DRAIN();
      STAMP(1);
      e1 = __builtin_amdgcn_readfirstlane(ep) + 1u;
    } else {
      __builtin_amdgcn_s_setprio(3);
      if (w == 4) sth(sent4, rw, W_QA + (((l >> 5) * H + hg4 + ((l >> 3) & 3)) * DC + 64 * cs) * 2 + (l & 7) * 16);
      DRAIN();
      e1 = __builtin_amdgcn_readfirstlane(ep) + 1u;
      if (w == 4 && l == 0) sth2(mktag(e1), rw, W_FLAG + ((y & 7) * 8 + cs) * 128);
      __builtin_amdgcn_s_setprio(0);
      asm volatile("" :: "v"(cpf));
      STAMP(1);
    }
    int oa = W_QA + (((g >> 1) * H + hg4 + w) * DC + 64 * cs + 32 * (g & 1) + 2 * r) * 2;
    int qw = 65536 + ((g >> 1) * 8 + w) * 272 + (8 * r + 4 * (g & 1)) * 2, qr = 65536 + (w & 3) * 4352 + r * 272 + 16 * g;
    int o = W_QP + (hg4 * 8 + w) * 1024 + l * 16;
    PIN(oa); PIN(qw); PIN(qr); PIN(o);
    RBAR();
    STAMP(2);
    {
      v4u u[4];
#pragma unroll
      for (int i = 0; i < 4; ++i) u[i] = ldhs(rw, o, i * 8192);
      SB();
#pragma unroll
      for (int t = 0; t < 4; ++t)
#pragma unroll
        for (int s = 0; s < 4; ++s) wk[t * 4 + s] = *(const LAS v4u*)(L + (32 * (t >> 1) + 2 * r + (t & 1)) * 1024 + (w & 3) * 256 + 16 * ((4 * s + g) ^ r));
#pragma unroll
      for (int i = 0; i < 16; ++i) PIN(wk[i]);
#pragma unroll 1
      for (u32 sp = 0; sp < SPIN_CAP; ++sp) {
        u32 bd = 0;
#pragma unroll
        for (int i = 0; i < 4; ++i) bd |= __any((u[i].y != e1) | (u[i].w != e1)) ? 1u << i : 0u;
        if (!bd) break;
        CBAR(); SB();
#pragma unroll
        for (int i = 0; i < 4; ++i) if (bd >> i & 1) u[i] = ldhs(rw, o, i * 8192);
      }
      STAMP(3);
#pragma unroll
      for (int i = 0; i < 4; ++i) *(LAS v2u*)(L + qw + i * 4352) = v2u{u[i].x, u[i].z};
    }
    LBAR();
    STAMP(4);
    if (w < 4) {
      v4u qf[4];
#pragma unroll
      for (int s = 0; s < 4; ++s) qf[s] = *(const LAS v4u*)(L + qr + 64 * s);
      f4 a[4];
#pragma unroll
      for (int t = 0; t < 4; ++t) a[t] = f4{0.f, 0.f, 0.f, 0.f};
#pragma unroll
      for (int s = 0; s < 4; ++s)
#pragma unroll
        for (int t = 0; t < 4; ++t) a[t] = mfma16(qf[s], wk[t * 4 + s], a[t]);
      float st[4];
#pragma unroll
      for (int t = 0; t < 4; ++t) st[t] = (a[t].x + a[t].y) + (a[t].z + a[t].w);
      sth1(guard(pk2(swap16add(st[0], st[2]), swap16add(st[1], st[3]))), rw, oa);
      STAMP(5);
    }
    RBAR();
    if (KNOB(2) == 1) CLEAR_BOXES(h * 2 + jj);
  } else {
    const int i2 = bid - 128;
    const int b = (i2 & 7) >> 2, ks = (i2 & 3) + 4 * (i2 >> 3), bk = b * 32 + ks;
    const int hh = l >> 5;
    constexpr int L_Q = 131072, L_MX = 0, L_LS = 1024, L_P = 2048, L_SL = 10240;
    if (KNOB(2) == 0) { e1 = __builtin_amdgcn_readfirstlane(ep) + 1u; CLEAR_BOXES(bk); }
    v4u fr[32];
    int sr_ = L_SL + (w & 3) * 8704 + (l >> 3) * 272 + 16 * (l & 7);
    int oo = W_OP + bk * 32768 + (l >> 3) * 1024 + (128 * (w & 3) + 8 * (l & 7)) * 2;
    if (__builtin_expect(w < 4, 1)) {
      const u16* src = p.ckv + ((size_t)b * SK + 128 * ks + 32 * w + g) * DC;
      e1 = __builtin_amdgcn_readfirstlane(ep) + 1u;
      asm volatile("" : "+s"(e1));
      HOLD(0);
      v2u fl;
#pragma unroll
      for (int n = 0; n < 32; ++n) {
        const int blk = n >> 3, r0 = 4 * (n & 7);
        dma(src + r0 * DC + 128 * blk + 8 * (r ^ ((g << 2) | (n & 3))), L + blk * 32768 + (32 * w + r0) * 256);
        if (n == 23) {
          SB();
          asm volatile("buffer_load_dwordx2 %0, %1, %2, 0 offen sc1" : "=v"(fl) : "v"(W_FLAG + (16 * w + (l >> 2)) * 128), "s"(rw) : "memory");
          SB();
        }
      }
      STAMP(1);
      const int row = 32 * w + (l & 31), sw = swz(row);
      VMWAIT(17);
      RBAR();
#pragma unroll
      for (int s = 0; s < 16; ++s) fr[s] = *(const LAS v4u*)(L + (s >> 3) * 32768 + row * 256 + 16 * ((2 * (s & 7) + hh) ^ sw));
      SB();
      asm volatile("s_waitcnt vmcnt(8)" : "+v"(fl) :: "memory");
      RBAR();
      {
        const v2u tag = mktag(e1);
        if (__any((fl.x != tag.x) | (fl.y != tag.y))) wait_flags(rw, 16 * w + (l >> 2), tag);
      }
      HOLD(11);
      v4u qv[8];
      const int qo = W_QA + b * 32768 + w * 8192 + l * 16;
#pragma unroll
      for (int i = 0; i < 8; ++i) qv[i] = ldhs(rw, qo, i * 1024);
      SB();
#pragma unroll
      for (int s = 0; s < 8; ++s) fr[16 + s] = *(const LAS v4u*)(L + 2 * 32768 + row * 256 + 16 * ((2 * s + hh) ^ sw));
      SB();
      VMWAIT(8);
      STAMP(2);
#pragma unroll
      for (int s = 0; s < 8; ++s) fr[24 + s] = *(const LAS v4u*)(L + 3 * 32768 + row * 256 + 16 * ((2 * s + hh) ^ sw));
      SB();
#pragma unroll 1
      for (u32 sp = 0; sp < SPIN_CAP; ++sp) {
        u32 mx = 0;
#pragma unroll
        for (int i = 0; i < 8; ++i) {
          mx = umax(mx, max4(qv[i]));
          *(LAS v4u*)(L + L_Q + (8 * w + i) * 1024 + 16 * (l ^ ((8 * w + i) & 15))) = qv[i];
        }
        if (!__any(mx == SENT)) break;
        CBAR();
#pragma unroll
        for (int i = 0; i < 8; ++i) qv[i] = ldhs(rw, qo, i * 1024);
      }
      STAMP(4);
      int qb = L_Q + (l & 31) * 1024;
      const int qx = (l & 15) ^ hh;
      int mxw = L_MX + ((l & 31) * 8 + w * 2 + hh) * 4;
      PIN(qb); PIN(mxw); PIN(sr_); PIN(oo);
#pragma unroll
      for (int s = 0; s < 32; ++s) PIN(fr[s]);
      STAMP(3);
      LBAR();
      STAMP(5);
      f16v sacc;
#pragma unroll
      for (int i = 0; i < 16; ++i) sacc[i] = 0.f;
      v4u qf[12];
#define QADDR(s) (L + qb + ((s) >> 3) * 256 + 16 * (((2 * (s)) & 15) ^ qx))
#pragma unroll
      for (int s = 0; s < 12; ++s) { qf[s] = *(const LAS v4u*)QADDR(s); SB(); }
#pragma unroll
      for (int s = 0; s < 32; ++s) {
        sacc = mfma32(fr[s], qf[s % 12], sacc);
        if (s + 12 < 32) qf[s % 12] = *(const LAS v4u*)QADDR(s + 12);
        SB();
      }
      float mx = fmaxf(fmaxf(sacc[0], sacc[1]), sacc[2]);
      PIN(mx); SB();
      mx = max3(max3(mx, sacc[3], sacc[4]), max3(sacc[5], sacc[6], sacc[7]), max3(max3(sacc[8], sacc[9], sacc[10]), max3(sacc[11], sacc[12], sacc[13]), max3(sacc[14], sacc[15], mx)));
      *(LAS float*)(L + mxw) = mx;
      LBAR();
      STAMP(6);
      const f4 m0 = *(const LAS f4*)(L + L_MX + (l & 31) * 32), m1 = *(const LAS f4*)(L + L_MX + (l & 31) * 32 + 16);
      mx = max3(max3(m0.x, m0.y, m0.z), max3(m0.w, m1.x, m1.y), max3(m1.z, m1.w, m1.w)) * SCL;
      float e[16];
#pragma unroll
      for (int i = 0; i < 16; ++i) e[i] = __builtin_amdgcn_exp2f(__builtin_fmaf(sacc[i], SCL, -mx));
      *(LAS v4u*)(L + L_P + ((2 * w) * 64 + l) * 16) = v4u{pk2(e[0], e[1]), pk2(e[2], e[3]), pk2(e[4], e[5]), pk2(e[6], e[7])};
      *(LAS v4u*)(L + L_P + ((2 * w + 1) * 64 + l) * 16) = v4u{pk2(e[8], e[9]), pk2(e[10], e[11]), pk2(e[12], e[13]), pk2(e[14], e[15])};
      float sum = (((e[0] + e[1]) + (e[2] + e[3])) + ((e[4] + e[5]) + (e[6] + e[7]))) + (((e[8] + e[9]) + (e[10] + e[11])) + ((e[12] + e[13]) + (e[14] + e[15])));
      *(LAS float*)(L + mxw + L_LS) = sum;
      LBAR();
      STAMP(7);
      if (w == 0 && l < 32) {
        const f4 s0 = *(const LAS f4*)(L + L_LS + l * 32), s1 = *(const LAS f4*)(L + L_LS + l * 32 + 16);
        sum = ((s0.x + s0.y) + (s0.z + s0.w)) + ((s1.x + s1.y) + (s1.z + s1.w));
        sth(v4u{__float_as_uint(mx), e1, __float_as_uint(sum), e1}, rw, W_ML + bk * 512 + l * 16);
      }
      if (KNOB(2) == 5 && w == 1 && l == 0) sth2(mktag(e1), rw, W_FLAG + (64 + bk) * 128);
      RBAR();
      v4u rb[4];
#pragma unroll
      for (int n = 0; n < 4; ++n) rb[n] = *(const LAS v4u*)(L + sr_ + n * 8 * 272);
#pragma unroll
      for (int n = 0; n < 4; ++n) sths(rb[n], rw, oo, n * 8192);
      RBAR();
    } else {
      const int w4 = w - 4;
      e1 = __builtin_amdgcn_readfirstlane(ep) + 1u;
      asm volatile("" :: "v"(cpf));
      const int bk2 = (l >> 4) & 1, q = (l & 15) >> 2, pp = l & 3;
#define VFRAGS() do { \
      _Pragma("unroll") for (int ct = 0; ct < 4; ++ct) _Pragma("unroll") for (int kk = 0; kk < 8; ++kk) { \
          const int r0 = 16 * kk + 4 * hh + q, r1 = r0 + 8, ch = 4 * ct + 2 * bk2 + (pp >> 1); \
          LAS unsigned char* base = L + w4 * 32768 + 8 * (pp & 1); \
          fr[ct * 8 + kk] = trfrag(base + r0 * 256 + 16 * (ch ^ swz(r0)), base + r1 * 256 + 16 * (ch ^ swz(r1))); } \
      _Pragma("unroll") for (int s = 0; s < 32; ++s) PIN(fr[s]); } while (0)
      const v2u tag = mktag(e1);
      v2u fl = tag;
      const int nb = w4 < 2 ? 1 : w4;
#pragma unroll 1
      for (int k = 0; k < nb; ++k) RBAR();
      if (w4 == 3) { STAMP(5); if (KNOB(2) != 5) fl = ldh2(rw, W_FLAG + (64 + bk) * 128); }
      SB();
      VFRAGS();
      if (w4 == 3 && __any((fl.x != tag.x) | (fl.y != tag.y))) wait_flags(rw, 64 + bk, tag);
      STAMP(3);
      asm volatile("s_waitcnt lgkmcnt(0)" ::: "memory");
#pragma unroll 1
      for (int k = nb; k < 3; ++k) RBAR();
      int sw_ = L_SL + w4 * 8704 + (l & 31) * 272 + 8 * hh;
      PIN(sw_); PIN(sr_); PIN(oo);
      if (KNOB(2) == 5) {
        if (w4 < 3) {
#pragma unroll
          for (int i = 0; i < 14; ++i) {
            const int u = w4 * 14 + i;
            sths(sent4, rw, l * 16, u < 32 ? W_OP + bk * 32768 + u * 1024 : W_PO + bk * 10240 + (u - 32) * 1024);
          }
        }
        STAMP(1);
      }
      RBAR();
      if (KNOB(2) == 5) { DRAIN(); STAMP(2); }
      RBAR();
      STAMP(7);
      v4u pf[8];
#pragma unroll
      for (int kk = 0; kk < 8; ++kk) pf[kk] = *(const LAS v4u*)(L + L_P + (kk * 64 + l) * 16);
      SB();
      f16v o[4];
#pragma unroll
      for (int ct = 0; ct < 5; ++ct) {
        if (ct < 4) {
#pragma unroll
          for (int i = 0; i < 16; ++i) o[ct][i] = 0.f;
        }
#pragma unroll
        for (int kk = 0; kk < 8; ++kk) {
          if (ct < 4) o[ct] = mfma32(fr[ct * 8 + kk], pf[kk], o[ct]);
          SB();
          if (ct > 0 && kk >= 1 && kk < 5) {
            const int g4 = kk - 1, pt = ct - 1;
            *(LAS v2u*)(L + sw_ + (32 * pt + 8 * g4) * 2) = v2u{guard(pk2(o[pt][4 * g4], o[pt][4 * g4 + 1])), guard(pk2(o[pt][4 * g4 + 2], o[pt][4 * g4 + 3]))};
          }
          if (ct == 2 && kk == 6) LBAR();
          SB();
        }
      }
      STAMP(6);
      LBAR();
    }
    {
      const int half = (w >> 2) * 2;
      const v4u r0 = *(const LAS v4u*)(L + sr_ + half * 8 * 272 + 128), r1 = *(const LAS v4u*)(L + sr_ + (half + 1) * 8 * 272 + 128);
      sth(r0, rw, oo + half * 8192 + 128);
      sth(r1, rw, oo + half * 8192 + 8192 + 128);
    }
    STAMP(8);
    if (i2 < 40) {
      const int du = i2, db = du / 20;
      float gate;
      {
        const rsrc_t rg = mk(p.gw, M * 2);
        float s = 0.f;
#pragma unroll
        for (int i = 0; i < 5; ++i) {
          const u32 xa = __builtin_amdgcn_raw_buffer_load_b32(rx, (db * M + 128 * i + 2 * l) * 2, 0, 0);
          const u32 ga = __builtin_amdgcn_raw_buffer_load_b32(rg, (128 * i + 2 * l) * 2, 0, 0);
          s += bflo(xa) * bflo(ga) + bfhi(xa) * bfhi(ga);
        }
        s += rorn<1>(s); s += rorn<2>(s); s += rorn<4>(s); s += rorn<8>(s);
        s = swap16add(s, s); s = swap32add(s, s);
        gate = 1.f / (1.f + __expf(-s));
      }
      const int pr = tid >> 3;
      wait_flags(rw, 64 + 4 * w + ((l >> 3) & 3) + 32 * (l >> 5), mktag(e1));
      int lw = ((4 * (l & 7) + g) * 8 + w) * 4;
      PIN(lw);
      LAS unsigned char* LD = L + 65536;
      STAMP(9);
      f4 v;
      {
        v4u t0, t1, f0, f1;
        const int o = W_PO + pr * 5120 + du * 128 + (l & 7) * 16;
        const int of = W_PF + (int)umin(pr, 53) * 10240 + du * 256 + (l & 7) * 32;
        bool fok = false;
#pragma unroll 1
        for (u32 sp = 0; sp < SPIN_CAP; ++sp) {
          CBAR();
          t0 = ldh(rw, o); t1 = ldhs(rw, o, 64 * 5120);
          if (!fok) {
            f0 = ldh(rw, of); f1 = ldh(rw, of + 16);
            fok = !__any((f0.y != e1) | (f0.w != e1) | (f1.y != e1) | (f1.w != e1));
          }
          if (fok && !__any(umax(max4(t0), max4(t1)) == SENT)) break;
        }
        const float gf = pr < 54 ? 1.f - gate : 0.f;
        v = f4{__uint_as_float(f0.x), __uint_as_float(f0.z), __uint_as_float(f1.x), __uint_as_float(f1.z)} * gf + (__builtin_bit_cast(f4, t0) + __builtin_bit_cast(f4, t1)) * gate;
      }
      STAMP(10);
      float s = swap32add(swap16add(v.x, v.y), swap16add(v.z, v.w));
      s += rorn<8>(s);
      *(LAS float*)(LD + lw) = s;
      LBAR();
      if (w == 0) {
        const f4 s0 = *(const LAS f4*)(LD + (l & 31) * 32), s1 = *(const LAS f4*)(LD + (l & 31) * 32 + 16);
        const float t = ((s0.x + s0.y) + (s0.z + s0.w)) + ((s1.x + s1.y) + (s1.z + s1.w));
        if (l < 32) p.out[32 * du + l] = (u16)pk2(t, t);
        if (du == 0 && l == 0) sth1(e1, rw, W_EPOCH);
      }
      STAMP(11);
    }
    goto done;
  }
  if (bid >= 128 || j < 2) {
    const int ac = h * 4 + j;
    HOLD(3);
    const int cj = j >> 1, dj = j & 1;
    {
      if (KNOB(7)) {
        const u16* sp = p.wuv + ((size_t)(256 * cj + 32 * ((h & 3) * 2 + dj) + 4 * w) * H + (h & ~3)) * D + 8 * l;
#pragma unroll
        for (int n = 0; n < 4; ++n) dma(sp + n * H * D, L + 131072 + (w * 4 + n) * 1024);
      }
      const u16* so = p.wo + ((size_t)h * D + 64 * dj + 8 * w + g) * M;
      const int fo = 2 * (g | ((w & 1) << 2));
#pragma unroll
      for (int n = 0; n < 10; ++n) dma(so + (n & 1) * 4 * M + (n >> 1) * 128 + 8 * (r ^ fo), L + 32768 + (n >> 1) * 16384 + (8 * w + 4 * (n & 1)) * 256);
      if (KNOB(7)) RBAR();
      const u16* sv = p.wuv + ((size_t)(256 * cj + 32 * w + (l >> 3)) * H + h) * D + 64 * dj;
#pragma unroll
      for (int n = 0; n < 4; ++n) {
        const int fz = 2 * (((l >> 4) & 1) | ((n & 1) << 1));
        dma(sv + n * 8 * H * D + 8 * ((l & 7) ^ fz), L + (w * 4 + n) * 1024);
      }
    }
    const int cb_b = w >> 2, cb = (w >> 1) & 1, kh = w & 1;
    constexpr int L_A2 = 114688, L_A3 = L_A2 + 4 * 528, L_WT = 118784;
    int a2w = L_A2 + (cb_b * 2 + kh) * 528 + (128 * cb + 8 * r + 4 * (g & 1)) * 2;
    int a2r = L_A2 + (r & 3) * 528 + 16 * g;
    int a3w = L_A3 + (g & 1) * 144 + (16 * (w & 3) + r) * 2;
    int a3r = L_A3 + (r >> 3) * 144 + 16 * g;
    int po = W_PO + ac * 5120 + ((g >> 1) * M + 32 * w + 16 * (g & 1) + r) * 4;
    int oo = W_OP + (((cb_b * 32 + 16 * kh + g) * H + h) * DC + 256 * cj + 128 * cb + 8 * r) * 2;
    PIN(a2w); PIN(a2r); PIN(a3w); PIN(a3r); PIN(po); PIN(oo);
    if (KNOB(2) != 5) wait_flags(rw, 64 + 8 * w + (l >> 3), mktag(e1));
    DRAIN();
    RBAR();
    STAMP(6);
    v4u of[4], ml = {0u, 0u, 0u, 0u};
    if (w == 7) ml = ldh(rw, W_ML + (l * H + h) * 16);
#pragma unroll
    for (int i = 0; i < 4; ++i) of[i] = KNOB(2) == 5 ? sent4 : ldhs(rw, oo, i * 4 * H * DC * 2);
    SB();
    v4u wv[8], wo[12];
    {
      const int q = r >> 2, pp = l & 3;
#pragma unroll
      for (int ks = 0; ks < 8; ++ks) {
        const int row = 32 * ks + 8 * g + q, fz = 2 * (((q >> 1) & 1) | ((g & 1) << 1));
        LAS unsigned char* a = L + row * 128 + 16 * ((2 * (w & 3) + (pp >> 1)) ^ fz) + 8 * (pp & 1);
        wv[ks] = trfrag(a, a + 512);
      }
#pragma unroll
      for (int pi = 0; pi < 3; ++pi) {
        const int P = (pi == 2 ? (w & 3) : w) + 8 * pi;
#pragma unroll
        for (int tt = 0; tt < 2; ++tt)
#pragma unroll
          for (int ks = 0; ks < 2; ++ks) {
            const int row = 32 * ks + 8 * g + q, fo = 2 * (q | ((g & 1) << 2));
            LAS unsigned char* a = L + 32768 + (P >> 2) * 16384 + row * 256 + 16 * ((4 * (P & 3) + 2 * tt + (pp >> 1)) ^ fo) + 8 * (pp & 1);
            wo[pi * 4 + tt * 2 + ks] = trfrag(a, a + 1024);
          }
      }
#pragma unroll
      for (int i = 0; i < 8; ++i) PIN(wv[i]);
#pragma unroll
      for (int i = 0; i < 12; ++i) PIN(wo[i]);
    }
    STAMP(11);
    if (w == 7) {
#pragma unroll 1
      for (u32 sp = 0; sp < SPIN_CAP; ++sp) {
        if (!__any((ml.y != e1) | (ml.w != e1))) break;
        CBAR();
        ml = ldh(rw, W_ML + (l * H + h) * 16);
      }
      STAMP(10);
      const float mm = __uint_as_float(ml.x), ll = __uint_as_float(ml.z);
      float mx = mm;
      mx = fmaxf(mx, rorn<1>(mx)); mx = fmaxf(mx, rorn<2>(mx)); mx = fmaxf(mx, rorn<4>(mx)); mx = fmaxf(mx, rorn<8>(mx));
      { auto t = __builtin_amdgcn_permlane16_swap(__float_as_uint(mx), __float_as_uint(mx), false, false); mx = fmaxf(__uint_as_float(t[0]), __uint_as_float(t[1])); }
      const float e = __builtin_amdgcn_exp2f(mm - mx);
      float sum = e * ll;
      sum += rorn<1>(sum); sum += rorn<2>(sum); sum += rorn<4>(sum); sum += rorn<8>(sum);
      sum = swap16add(sum, sum);
      const float wgt = e * __builtin_amdgcn_rcpf(sum);
      *(LAS u32*)(L + L_WT + ((l & 48) + (l & 3) * 4 + ((l >> 2) & 3)) * 4) = pk2(wgt, wgt);
    }
    LBAR();
    STAMP(7);
    v4u wsel[4];
    {
      const v4u wt = *(const LAS v4u*)(L + L_WT + (cb_b * 32 + kh * 16 + g * 4) * 4);
      bool bad = __any(umax(umax(max4(of[0]), max4(of[1])), umax(max4(of[2]), max4(of[3]))) == SENT);
      if (bad) {
#pragma unroll
        for (int i = 0; i < 4; ++i) of[i] = ldhs(rw, oo, i * 4 * H * DC * 2);
      }
      SB();
      wsel[0] = selfrag(wt.x, mask); wsel[1] = selfrag(wt.y, mask); wsel[2] = selfrag(wt.z, mask); wsel[3] = selfrag(wt.w, mask);
#pragma unroll
      for (int i = 0; i < 4; ++i) PIN(wsel[i]);
      SB();
#pragma unroll 1
      for (u32 sp = 0; sp < SPIN_CAP; ++sp) {
        u32 bd = 0;
#pragma unroll
        for (int i = 0; i < 4; ++i) bd |= __any(max4(of[i]) == SENT) ? 1u << i : 0u;
        if (!bd) break;
        CBAR(); SB();
#pragma unroll
        for (int i = 0; i < 4; ++i) if (bd >> i & 1) of[i] = ldhs(rw, oo, i * 4 * H * DC * 2);
      }
    }
    STAMP(8);
    {
      f4 acc = {0.f, 0.f, 0.f, 0.f};
#pragma unroll
      for (int i = 0; i < 4; ++i) acc = mfma16(wsel[i], of[i], acc);
      *(LAS v2u*)(L + a2w) = v2u{pk2(acc.x, acc.y), pk2(acc.z, acc.w)};
    }
    LBAR();
    if (w < 4) {
      v4u af[8];
#pragma unroll
      for (int ks = 0; ks < 8; ++ks) af[ks] = *(const LAS v4u*)(L + a2r + 64 * ks);
      SB();
      f4 acc = {0.f, 0.f, 0.f, 0.f};
#pragma unroll
      for (int ks = 0; ks < 8; ++ks) acc = mfma16(af[ks], wv[ks], acc);
      const float v = (g & 1) ? acc.z + acc.w : acc.x + acc.y;
      *(LAS u16*)(L + a3w) = (u16)pk2(v, v);
    }
    LBAR();
    {
      const v4u a0 = *(const LAS v4u*)(L + a3r), a1 = *(const LAS v4u*)(L + a3r + 64);
#pragma unroll
      for (int pi = 0; pi < 3; ++pi) {
        if (pi == 2 && w >= 4) break;
        f4 t0 = {0.f, 0.f, 0.f, 0.f}, t1 = {0.f, 0.f, 0.f, 0.f};
        t0 = mfma16(a0, wo[pi * 4 + 0], t0); t1 = mfma16(a0, wo[pi * 4 + 2], t1);
        t0 = mfma16(a1, wo[pi * 4 + 1], t0); t1 = mfma16(a1, wo[pi * 4 + 3], t1);
        sth1(guard(__float_as_uint((g & 1) ? t1.x : t0.x)), rw, po + pi * 1024);
      }
    }
    STAMP(9);
  } else {
    const int i3 = h * 2 + j - 2;
    HOLD(4);
    if (i3 < 54) {
      const int pair = i3 >> 1, half = i3 & 1;
      STAMP(6);
      u32 xv[5];
#pragma unroll
      for (int f = 0; f < 5; ++f) xv[f] = __builtin_amdgcn_raw_buffer_load_b32(rx, (bsel * M + 320 * half + 40 * w + 10 * g) * 2 + 4 * f, 0, 0);
      v4u wf[10], wdn[8];
      const GAS u16* src = (const GAS u16*)((r >> 3) ? p.wu : p.wg) + (320 * half + 40 * w + 10 * g) * F + 64 * pair + 8 * (r & 7);
      if (w >= 1 && w < 6) {
        const rsrc_t rd = mk(p.wd, F * M * 2);
        const int vo = ((32 * i3 + 8 * g) * M + 128 * (w - 1) + 8 * r) * 2;
#pragma unroll
        for (int f = 0; f < 8; ++f) { if (f < KNOB(10)) THR(8); wdn[f] = ldw(rd, vo, f * M * 2); }
      } else {
#pragma unroll
        for (int f = 0; f < 8; ++f) wdn[f] = v4u{0u, 0u, 0u, 0u};
      }
      SB();
#pragma unroll
      for (int f = 0; f < 10; ++f) { if (f < KNOB(10) && (w == 0 || w >= 6)) THR(8); wf[f] = __builtin_nontemporal_load((const GAS v4u*)(src + f * F)); SB(); }
      f4 acc = {0.f, 0.f, 0.f, 0.f};
#pragma unroll
      for (int f = 0; f < 5; ++f) {
        acc = mfma16(selfrag(duplo(xv[f]), mask), wf[2 * f], acc);
        acc = mfma16(selfrag(duphi(xv[f]), mask), wf[2 * f + 1], acc);
      }
      *(LAS f4*)(L + (w * 64 + l) * 16) = acc;
      STAMP(7);
      LBAR();
      if (w == 0) {
        f4 s = *(const LAS f4*)(L + l * 16);
#pragma unroll
        for (int i = 1; i < 8; ++i) s += *(const LAS f4*)(L + (i * 64 + l) * 16);
        sth(v4u{__float_as_uint(s.x), e1, __float_as_uint(s.y), e1}, rw, W_FX + i3 * 2048 + l * 16);
        sth(v4u{__float_as_uint(s.z), e1, __float_as_uint(s.w), e1}, rw, W_FX + i3 * 2048 + 1024 + l * 16);
        v4u z0, z1;
#pragma unroll 1
        for (u32 sp = 0; sp < SPIN_CAP; ++sp) {
          CBAR();
          z0 = ldh(rw, W_FX + (i3 ^ 1) * 2048 + l * 16); z1 = ldh(rw, W_FX + (i3 ^ 1) * 2048 + 1024 + l * 16);
          if (!__any((z0.y != e1) | (z0.w != e1) | (z1.y != e1) | (z1.w != e1))) break;
        }
        const f4 o = {__uint_as_float(z0.x), __uint_as_float(z0.z), __uint_as_float(z1.x), __uint_as_float(z1.z)};
        s += o;
        float hv[4];
#pragma unroll
        for (int i = 0; i < 4; ++i) { const float gs = s[i], us = rorn<8>(s[i]); hv[i] = gs * __builtin_amdgcn_rcpf(1.f + __expf(-gs)) * us; }
        if ((r >> 2) == half) *(LAS v2u*)(L + 8192 + (g >> 1) * 64 + (8 * (r & 3) + 4 * (g & 1)) * 2) = v2u{pk2(hv[0], hv[1]), pk2(hv[2], hv[3])};
      }
      LBAR();
      if (w >= 1 && w < 6) {
        const v4u z = *(const LAS v4u*)(L + 8192 + bsel * 64 + g * 16);
        const u32 zz[4] = {z.x, z.y, z.z, z.w};
        acc = f4{0.f, 0.f, 0.f, 0.f};
#pragma unroll
        for (int f = 0; f < 4; ++f) {
          acc = mfma16(selfrag(duplo(zz[f]), mask), wdn[2 * f], acc);
          acc = mfma16(selfrag(duphi(zz[f]), mask), wdn[2 * f + 1], acc);
        }
        const int o = W_PF + i3 * 10240 + ((g >> 1) * M + 128 * (w - 1) + 8 * r + 4 * (g & 1)) * 8;
        sth(v4u{__float_as_uint(acc.x), e1, __float_as_uint(acc.y), e1}, rw, o);
        sth(v4u{__float_as_uint(acc.z), e1, __float_as_uint(acc.w), e1}, rw, o + 16);
      }
      STAMP(8);
    }
  }
done:;
}

extern "C" void kernel_launch(void* const* d_in, const int* in_sizes, int n_in,
                              void* d_out, int out_size, void* d_ws, size_t ws_size,
                              hipStream_t stream) {
  Params p;
  p.x = (const u16*)d_in[0]; p.ckv = (const u16*)d_in[1]; p.wq = (const u16*)d_in[2]; p.wuk = (const u16*)d_in[3];
  p.wuv = (const u16*)d_in[4]; p.wo = (const u16*)d_in[5]; p.wg = (const u16*)d_in[7]; p.wu = (const u16*)d_in[8];
  p.wd = (const u16*)d_in[9]; p.gw = (const u16*)d_in[10]; p.out = (u16*)d_out; p.ws = (unsigned char*)d_ws + WS_OFF(ws_size);
  (void)hipFuncSetAttribute((const void*)mk_kernel, hipFuncAttributeMaxDynamicSharedMemorySize, LDS_BYTES);
  mk_kernel<<<256, NT, LDS_BYTES, stream>>>(p);
}
```

```cpp
#include <hip/hip_runtime.h>
#include <stdint.h>

#define STAMPS 0
#define WS_OFF(n) 0
#define TUNE 0
#define KNOB(i) kKnob[i]
#define KSET 0, 0, 5, 0, 0, 2, 0, 0, 0, 0, 0, 0
constexpr int kKnob[12] = {KSET};

typedef unsigned short u16;
typedef unsigned int u32;
typedef unsigned long long u64;
typedef u32 v4u __attribute__((ext_vector_type(4)));
typedef u32 v2u __attribute__((ext_vector_type(2)));
typedef float f4 __attribute__((ext_vector_type(4)));
typedef float f2 __attribute__((ext_vector_type(2)));
typedef float f16v __attribute__((ext_vector_type(16)));
typedef short bf8 __attribute__((ext_vector_type(8)));
typedef short s4 __attribute__((ext_vector_type(4)));
typedef __bf16 bf2 __attribute__((ext_vector_type(2)));
#define GAS __attribute__((address_space(1)))
#define LAS __attribute__((address_space(3)))
#define DI __device__ __forceinline__
typedef __amdgpu_buffer_rsrc_t rsrc_t;

constexpr int NB = 2, H = 32, SK = 4096, D = 128, M = 640, DC = 512, F = 1728;
constexpr int NT = 512;
constexpr int W_EPOCH = 0;
constexpr int W_FLAG = 4096;
constexpr int W_QA = 65536;
constexpr int W_ML = 24576;
constexpr int W_FX = 131072;
constexpr int W_QP = 262144;
constexpr int W_PO = 524288;
constexpr int W_PF = 1179648;
constexpr int W_OP = 2097152;
constexpr int W_END = 4194304;
constexpr int W_DBG = 8388608;
constexpr int LDS_BYTES = 163840;
constexpr int SC1 = 16, NTL = 2;
constexpr u32 SENT = 0xFFFFFFFFu;
constexpr u32 SPIN_CAP = 1u << 20;
#define CODE_PF 16896
constexpr int CODE_PF_BYTES = CODE_PF;
constexpr float SCL = 0.08838834764831845f * 1.4426950408889634f;

struct Params {
  const u16* x; const u16* ckv; const u16* wq; const u16* wuk; const u16* wuv; const u16* wo;
  const u16* wg; const u16* wu; const u16* wd; const u16* gw; u16* out; unsigned char* ws;
};

DI rsrc_t mk(const void* p, int bytes) { return __builtin_amdgcn_make_buffer_rsrc((void*)p, 0, bytes, 0x00020000); }
DI v4u ldw(rsrc_t r, int voff, int soff) { return __builtin_amdgcn_raw_buffer_load_b128(r, voff, soff, NTL); }
DI v4u ldh(rsrc_t r, int voff) { return __builtin_amdgcn_raw_buffer_load_b128(r, voff, 0, SC1); }
DI v4u ldhs(rsrc_t r, int voff, int soff) { return __builtin_amdgcn_raw_buffer_load_b128(r, voff, soff, SC1); }
DI v2u ldh2(rsrc_t r, int voff) { return __builtin_amdgcn_raw_buffer_load_b64(r, voff, 0, SC1); }
DI void sth(v4u v, rsrc_t r, int voff) { __builtin_amdgcn_raw_buffer_store_b128(v, r, voff, 0, SC1); }
DI void sths(v4u v, rsrc_t r, int voff, int soff) { __builtin_amdgcn_raw_buffer_store_b128(v, r, voff, soff, SC1); }
DI void sth2(v2u v, rsrc_t r, int voff) { __builtin_amdgcn_raw_buffer_store_b64(v, r, voff, 0, SC1); }
DI void sth1(u32 v, rsrc_t r, int voff) { __builtin_amdgcn_raw_buffer_store_b32(v, r, voff, 0, SC1); }
DI u32 umin(u32 a, u32 b) { return a < b ? a : b; }
DI u32 umax(u32 a, u32 b) { return a > b ? a : b; }
DI u32 guard(u32 v) { return umin(v, 0xFFFFFFFEu); }
DI v4u guard4(v4u v) { return v4u{guard(v.x), guard(v.y), guard(v.z), guard(v.w)}; }
DI u32 max4(v4u v) { return umax(umax(v.x, v.y), umax(v.z, v.w)); }
DI u32 pk2(float lo, float hi) { bf2 r = {(__bf16)lo, (__bf16)hi}; return __builtin_bit_cast(u32, r); }
DI float bflo(u32 v) { return __uint_as_float(v << 16); }
DI float bfhi(u32 v) { return __uint_as_float(v & 0xFFFF0000u); }
DI f4 mfma16(v4u a, v4u b, f4 c) { return __builtin_amdgcn_mfma_f32_16x16x32_bf16(__builtin_bit_cast(bf8, a), __builtin_bit_cast(bf8, b), c, 0, 0, 0); }
DI f16v mfma32(v4u a, v4u b, f16v c) { return __builtin_amdgcn_mfma_f32_32x32x16_bf16(__builtin_bit_cast(bf8, a), __builtin_bit_cast(bf8, b), c, 0, 0, 0); }
#define CBAR() asm volatile("" ::: "memory")
#define SB() __builtin_amdgcn_sched_barrier(0)
#define PIN(x) asm volatile("" : "+v"(x))
#define DRAIN() asm volatile("s_waitcnt vmcnt(0)" ::: "memory")
#define VMWAIT(n) asm volatile("s_waitcnt vmcnt(" #n ")" ::: "memory")
#define THR(i) do { const int d_ = KNOB(i); if (d_ == 1) VMWAIT(0); else if (d_ == 2) VMWAIT(1); else if (d_ == 3) VMWAIT(2); else if (d_ == 4) VMWAIT(3); else if (d_ == 6) VMWAIT(5); else if (d_ == 8) VMWAIT(7); SB(); } while (0)
#define RBAR() do { CBAR(); __builtin_amdgcn_s_barrier(); CBAR(); } while (0)
#define LBAR() do { asm volatile("s_waitcnt lgkmcnt(0)" ::: "memory"); __builtin_amdgcn_s_barrier(); CBAR(); } while (0)
template <int N> DI float rorn(float v) { return __builtin_bit_cast(float, __builtin_amdgcn_update_dpp(0, __builtin_bit_cast(int, v), 0x120 + N, 0xf, 0xf, false)); }
DI float max3(float a, float b, float c) { float d; asm("v_max3_f32 %0, %1, %2, %3" : "=v"(d) : "v"(a), "v"(b), "v"(c)); return d; }
DI float swap16add(float x, float y) { auto t = __builtin_amdgcn_permlane16_swap(__float_as_uint(x), __float_as_uint(y), false, false); return __uint_as_float(t[0]) + __uint_as_float(t[1]); }
DI float swap32add(float x, float y) { auto t = __builtin_amdgcn_permlane32_swap(__float_as_uint(x), __float_as_uint(y), false, false); return __uint_as_float(t[0]) + __uint_as_float(t[1]); }
DI v4u selfrag(u32 vv, v4u mask) { return v4u{vv & mask.x, vv & mask.y, vv & mask.z, vv & mask.w}; }
DI u32 duplo(u32 v) { return __builtin_amdgcn_perm(v, v, 0x01000100u); }
DI u32 duphi(u32 v) { return __builtin_amdgcn_perm(v, v, 0x03020302u); }
DI int swz(int row) { return ((row & 3) << 2) | ((row >> 2) & 3); }
DI void dma(const u16* src, LAS unsigned char* dst) { __builtin_amdgcn_global_load_lds((const GAS u32*)src, (LAS u32*)dst, 16, 0, NTL); }
DI void dma_asm(const u16* src, u32 lds_addr) {
  u32 keep;
  asm volatile("s_mov_b32 %0, m0\n\ts_mov_b32 m0, %2\n\ts_nop 0\n\tglobal_load_lds_dwordx4 %1, off nt\n\ts_mov_b32 m0, %0" : "=&s"(keep) : "v"(src), "s"(lds_addr) : "memory");
}
DI v4u trfrag(LAS unsigned char* a0, LAS unsigned char* a1) {
  const v2u lo = __builtin_bit_cast(v2u, __builtin_amdgcn_ds_read_tr16_b64_v4i16((LAS s4*)a0));
  const v2u hi = __builtin_bit_cast(v2u, __builtin_amdgcn_ds_read_tr16_b64_v4i16((LAS s4*)a1));
  return v4u{lo.x, lo.y, hi.x, hi.y};
}
DI v2u mktag(u32 e1) { return v2u{e1, e1 * 0x9E3779B1u ^ 0x5bd1e995u}; }
DI u32 epoch1(rsrc_t rw) { return __builtin_amdgcn_readfirstlane(__builtin_amdgcn_raw_buffer_load_b32(rw, W_EPOCH, 0, SC1)) + 1u; }

#define ROLE_OF(b) ((b) < 128 ? ((((b) >> 3) & 2) ? 2 : 0) : (b) < 192 ? 1 : 3)
#define NROLES 4
#define STAMP(k) do { if ((k) == 99 && (0 < 0 || ROLE_OF(bid) == 0 || (0 == 0 && ROLE_OF(bid) == 3 && (k) >= 6) || (0 == 0 && ROLE_OF(bid) == 2 && (k) <= 4))) { const u64 t_ = __builtin_amdgcn_s_memrealtime(); if (l == 0) __hip_atomic_store((GAS u64*)(p.ws + W_DBG) + 256 + bid * 8 + w, t_, __ATOMIC_RELAXED, __HIP_MEMORY_SCOPE_AGENT); } } while (0)

DI void wait_flags(rsrc_t rw, int idx, v2u tag) {
#pragma unroll 1
  for (u32 sp = 0; sp < SPIN_CAP; ++sp) {
    CBAR();
    v2u f = ldh2(rw, W_FLAG + idx * 128);
    if (!__any((f.x != tag.x) | (f.y != tag.y))) break;
    __builtin_amdgcn_s_sleep(1);
  }
  CBAR();
}

__global__ void __launch_bounds__(NT, 2) mk_kernel(Params p) {
  extern __shared__ __attribute__((aligned(16))) unsigned char smem[];
  LAS unsigned char* L = (LAS unsigned char*)smem;
  const int tid = threadIdx.x, l = tid & 63, w = __builtin_amdgcn_readfirstlane(tid >> 6);
  const int bid = blockIdx.x;
  const int r = l & 15, g = l >> 4;
  STAMP(0);
  const u64 t_start = __builtin_amdgcn_s_memrealtime();
#define HOLD(i) do { if (KNOB(i) > 0) while (__builtin_amdgcn_s_memrealtime() - t_start < (u64)KNOB(i)) __builtin_amdgcn_s_sleep(1); } while (0)
  u64 pc0; asm volatile("s_getpc_b64 %0" : "=s"(pc0));
  u32 cpf = 0;
  if (KNOB(9) == 0) { if (w >= 5) cpf = ((const GAS u32*)(pc0 & ~127ull))[umin(tid - 320, CODE_PF_BYTES / 128 - 1) * 32]; }
  else if (KNOB(9) == 1) { if (w >= 3) cpf = __builtin_nontemporal_load((const GAS u32*)(pc0 & ~127ull) + umin(tid - 192, CODE_PF_BYTES / 64 - 1) * 16); }
  else if (KNOB(9) == 2) { if (w >= 3) cpf = ((const GAS u32*)(pc0 & ~127ull))[umin(tid - 192, CODE_PF_BYTES / 64 - 1) * 16]; }
  CBAR(); SB();
  if (__builtin_expect(bid >= 192 && w < 4, 1)) {
    const u16* src = p.wuk + ((size_t)(256 * ((bid >> 3) & 1) + 64 * w + g) * H + (bid & 7) * 4 + ((bid - 192) >> 4)) * D;
#pragma unroll 1
    for (int n = 0; n < 16; ++n) dma_asm(src + n * 4 * H * D + 8 * (r ^ ((2 * n + (g >> 1)) & 15)), (u32)(size_t)L + w * 16384 + n * 1024);
  }
  SB();
  const rsrc_t rw = mk(p.ws, W_END);
  const u32 ep = __builtin_amdgcn_raw_buffer_load_b32(rw, W_EPOCH, 0, SC1);
  SB();
  if (w == 7) { if (l < 8) __hip_atomic_store((GAS u64*)(p.ws + W_DBG) + 256 + bid * 8 + l, t_start, __ATOMIC_RELAXED, __HIP_MEMORY_SCOPE_AGENT); if (l == 8) __hip_atomic_store((GAS u64*)(p.ws + W_DBG) + bid, t_start, __ATOMIC_RELAXED, __HIP_MEMORY_SCOPE_AGENT); }
  const rsrc_t rx = mk(p.x, NB * M * 2);
  const u32 m16 = (l & 1) ? 0xFFFF0000u : 0x0000FFFFu;
  const int jq = (l & 7) >> 1;
  const v4u mask = {jq == 0 ? m16 : 0u, jq == 1 ? m16 : 0u, jq == 2 ? m16 : 0u, jq == 3 ? m16 : 0u};
  const int bsel = (l >> 3) & 1;
  const v4u sent4 = {SENT, SENT, SENT, SENT};
  int h, j;
  u32 e1;
#define CLEAR_BOXES(bx) do { \
    _Pragma("unroll") for (int i = 0; i < 4; ++i) sths(sent4, rw, W_OP + (bx) * 32768 + tid * 16, i * 8192); \
    sths(sent4, rw, W_PO + (bx) * 10240 + tid * 16, 0); \
    if (w < 2) sths(sent4, rw, W_PO + (bx) * 10240 + tid * 16, 8192); \
    DRAIN(); RBAR(); \
    if (tid == 0) sth2(mktag(e1), rw, W_FLAG + (64 + (bx)) * 128); } while (0)

  if (__builtin_expect(bid < 128, 1)) {
    h = (bid & 7) * 4 + (bid >> 5); j = (bid >> 3) & 3;
    const int x = h * 4 + j;
    if (__builtin_expect(w < 2, 1)) {
      const rsrc_t rq = mk(p.wq, M * H * D * 2);
      const int mb = 160 * j + 80 * w + 20 * g;
      u32 xv[10];
#pragma unroll
      for (int f = 0; f < 10; ++f) xv[f] = __builtin_amdgcn_raw_buffer_load_b32(rx, (bsel * M + mb) * 2 + 4 * f, 0, 0);
      v4u wa[20];
      const int vo = (mb * (H * D) + h * D + 8 * r) * 2;
#pragma unroll
      for (int f = 0; f < 20; ++f) { wa[f] = ldw(rq, vo, f * H * D * 2); SB(); }
      STAMP(1);
      SB();
      v4u sel[20];
#pragma unroll
      for (int f = 0; f < 10; ++f) { sel[2 * f] = selfrag(duplo(xv[f]), mask); sel[2 * f + 1] = selfrag(duphi(xv[f]), mask); }
#pragma unroll
      for (int f = 0; f < 20; ++f) PIN(sel[f]);
      e1 = __builtin_amdgcn_readfirstlane(ep) + 1u;
      int o = W_QP + (x * 2 + w) * 1024 + l * 16;
      PIN(o);
      SB();
      f4 acc = {0.f, 0.f, 0.f, 0.f};
#pragma unroll
      for (int f = 0; f < 20; ++f) acc = mfma16(sel[f], wa[f], acc);
      sth(v4u{pk2(acc.x, acc.y), e1, pk2(acc.z, acc.w), e1}, rw, o);
      STAMP(3);
    } else {
      e1 = __builtin_amdgcn_readfirstlane(ep) + 1u;
      asm volatile("" :: "v"(cpf));
    }
    RBAR();
    HOLD(6);
    if (KNOB(2) == 2 && j >= 2) CLEAR_BOXES(h * 2 + j - 2);
    if (KNOB(2) == 4 && j < 2) CLEAR_BOXES(h * 2 + j);
    if (KNOB(5) > 0 && (KNOB(1) == 0 || j < 2)) {
      if (KNOB(1) == 0) {
        const int i2 = (bid & 7) + 8 * (bid >> 4), b = (i2 & 7) >> 2, ks = (i2 & 3) + 4 * (i2 >> 3);
        const u16* src = p.ckv + ((size_t)b * SK + 128 * ks + 64 * ((bid >> 3) & 1) + 8 * w + g) * DC + 8 * r;
#pragma unroll
        for (int blk = 4 - KNOB(5); blk < 4; ++blk) { dma(src + 128 * blk, L + 131072 + w * 2048); dma(src + 128 * blk + 4 * DC, L + 131072 + w * 2048 + 1024); }
      } else {
        const int i2 = (bid & 15) + 16 * (bid >> 5), b = (i2 & 7) >> 2, ks = (i2 & 3) + 4 * (i2 >> 3);
        const u16* src = p.ckv + ((size_t)b * SK + 128 * ks + 16 * w + g) * DC + 8 * r;
#pragma unroll
        for (int blk = 4 - KNOB(5); blk < 4; ++blk)
#pragma unroll
          for (int n = 0; n < 4; ++n) dma(src + 128 * blk + 4 * n * DC, L + 131072 + w * 4096 + n * 1024);
      }
      RBAR();
    }
    if (KNOB(2) == 3 && j >= 2) CLEAR_BOXES(h * 2 + j - 2);
    STAMP(4);
  } else if (bid >= 192) {
    const int y = bid - 192;
    h = (y & 7) * 4 + (y >> 4);
    const int jj = (y >> 3) & 1;
    j = 2 + jj;
    v4u wk[16];
    if (w < 4) {
      DRAIN();
      STAMP(1);
      e1 = __builtin_amdgcn_readfirstlane(ep) + 1u;
    } else {
      __builtin_amdgcn_s_setprio(3);
      if (w == 4) sth(sent4, rw, W_QA + (((l >> 5) * H + h) * DC + 256 * jj) * 2 + (l & 31) * 16);
      DRAIN();
      e1 = __builtin_amdgcn_readfirstlane(ep) + 1u;
      if (w == 4 && l == 0) sth2(mktag(e1), rw, W_FLAG + (h * 2 + jj) * 128);
      __builtin_amdgcn_s_setprio(0);
      asm volatile("" :: "v"(cpf));
      STAMP(1);
    }
    int oa = W_QA + (((g >> 1) * H + h) * DC + 256 * jj + 64 * w + 32 * (g & 1) + 2 * r) * 2;
    int qw = 65536 + ((g >> 1) * 8 + w) * 272 + (8 * r + 4 * (g & 1)) * 2, qr = 65536 + r * 272 + 16 * g;
    int o = W_QP + (h * 8 + w) * 1024 + l * 16;
    PIN(oa); PIN(qw); PIN(qr); PIN(o);
    RBAR();
    STAMP(2);
    {
      v4u u = ldh(rw, o);
      SB();
#pragma unroll
      for (int t = 0; t < 4; ++t)
#pragma unroll
        for (int s = 0; s < 4; ++s) wk[t * 4 + s] = *(const LAS v4u*)(L + (w & 3) * 16384 + (32 * (t >> 1) + 2 * r + (t & 1)) * 256 + 16 * ((4 * s + g) ^ r));
#pragma unroll
      for (int i = 0; i < 16; ++i) PIN(wk[i]);
#pragma unroll 1
      for (u32 sp = 0; sp < SPIN_CAP; ++sp) {
        if (!__any((u.y != e1) | (u.w != e1))) break;
        CBAR();
        u = ldh(rw, o);
      }
      STAMP(3);
      *(LAS v2u*)(L + qw) = v2u{u.x, u.z};
    }
    LBAR();
    STAMP(4);
    if (w < 4) {
      v4u qf[4];
#pragma unroll
      for (int s = 0; s < 4; ++s) qf[s] = *(const LAS v4u*)(L + qr + 64 * s);
      f4 a[4];
#pragma unroll
      for (int t = 0; t < 4; ++t) a[t] = f4{0.f, 0.f, 0.f, 0.f};
#pragma unroll
      for (int s = 0; s < 4; ++s)
#pragma unroll
        for (int t = 0; t < 4; ++t) a[t] = mfma16(qf[s], wk[t * 4 + s], a[t]);
      float st[4];
#pragma unroll
      for (int t = 0; t < 4; ++t) st[t] = (a[t].x + a[t].y) + (a[t].z + a[t].w);
      sth1(guard(pk2(swap16add(st[0], st[2]), swap16add(st[1], st[3]))), rw, oa);
      STAMP(5);
    }
    RBAR();
    if (KNOB(2) == 1) CLEAR_BOXES(h * 2 + jj);
  } else {
    const int i2 = bid - 128;
    const int b = (i2 & 7) >> 2, ks = (i2 & 3) + 4 * (i2 >> 3), bk = b * 32 + ks;
    const int hh = l >> 5;
    constexpr int L_Q = 131072, L_MX = 0, L_LS = 1024, L_P = 2048, L_SL = 10240;
    if (KNOB(2) == 0) { e1 = __builtin_amdgcn_readfirstlane(ep) + 1u; CLEAR_BOXES(bk); }
    v4u fr[32];
    int sr_ = L_SL + (w & 3) * 8704 + (l >> 3) * 272 + 16 * (l & 7);
    int oo = W_OP + bk * 32768 + (l >> 3) * 1024 + (128 * (w & 3) + 8 * (l & 7)) * 2;
    if (__builtin_expect(w < 4, 1)) {
      const u16* src = p.ckv + ((size_t)b * SK + 128 * ks + 32 * w + g) * DC;
      e1 = __builtin_amdgcn_readfirstlane(ep) + 1u;
      asm volatile("" : "+s"(e1));
      HOLD(0);
      v2u fl;
#pragma unroll
      for (int n = 0; n < 32; ++n) {
        const int blk = n >> 3, r0 = 4 * (n & 7);
        dma(src + r0 * DC + 128 * blk + 8 * (r ^ ((g << 2) | (n & 3))), L + blk * 32768 + (32 * w + r0) * 256);
        if (n == 23) {
          SB();
          asm volatile("buffer_load_dwordx2 %0, %1, %2, 0 offen sc1" : "=v"(fl) : "v"(W_FLAG + (16 * w + (l >> 2)) * 128), "s"(rw) : "memory");
          SB();
        }
      }
      STAMP(1);
      const int row = 32 * w + (l & 31), sw = swz(row);
      VMWAIT(17);
      RBAR();
#pragma unroll
      for (int s = 0; s < 16; ++s) fr[s] = *(const LAS v4u*)(L + (s >> 3) * 32768 + row * 256 + 16 * ((2 * (s & 7) + hh) ^ sw));
      SB();
      asm volatile("s_waitcnt vmcnt(8)" : "+v"(fl) :: "memory");
      RBAR();
      {
        const v2u tag = mktag(e1);
        if (__any((fl.x != tag.x) | (fl.y != tag.y))) wait_flags(rw, 16 * w + (l >> 2), tag);
      }
      HOLD(11);
      v4u qv[8];
      const int qo = W_QA + b * 32768 + w * 8192 + l * 16;
#pragma unroll
      for (int i = 0; i < 8; ++i) qv[i] = ldhs(rw, qo, i * 1024);
      SB();
#pragma unroll
      for (int s = 0; s < 8; ++s) fr[16 + s] = *(const LAS v4u*)(L + 2 * 32768 + row * 256 + 16 * ((2 * s + hh) ^ sw));
      SB();
      VMWAIT(8);
      STAMP(2);
#pragma unroll
      for (int s = 0; s < 8; ++s) fr[24 + s] = *(const LAS v4u*)(L + 3 * 32768 + row * 256 + 16 * ((2 * s + hh) ^ sw));
      SB();
#pragma unroll 1
      for (u32 sp = 0; sp < SPIN_CAP; ++sp) {
        u32 mx = 0;
#pragma unroll
        for (int i = 0; i < 8; ++i) {
          mx = umax(mx, max4(qv[i]));
          *(LAS v4u*)(L + L_Q + (8 * w + i) * 1024 + 16 * (l ^ ((8 * w + i) & 15))) = qv[i];
        }
        if (!__any(mx == SENT)) break;
        CBAR();
#pragma unroll
        for (int i = 0; i < 8; ++i) qv[i] = ldhs(rw, qo, i * 1024);
      }
      STAMP(4);
      int qb = L_Q + (l & 31) * 1024;
      const int qx = (l & 15) ^ hh;
      int mxw = L_MX + ((l & 31) * 8 + w * 2 + hh) * 4;
      PIN(qb); PIN(mxw); PIN(sr_); PIN(oo);
#pragma unroll
      for (int s = 0; s < 32; ++s) PIN(fr[s]);
      STAMP(3);
      LBAR();
      STAMP(5);
      f16v sacc;
#pragma unroll
      for (int i = 0; i < 16; ++i) sacc[i] = 0.f;
      v4u qf[12];
#define QADDR(s) (L + qb + ((s) >> 3) * 256 + 16 * (((2 * (s)) & 15) ^ qx))
#pragma unroll
      for (int s = 0; s < 12; ++s) { qf[s] = *(const LAS v4u*)QADDR(s); SB(); }
#pragma unroll
      for (int s = 0; s < 32; ++s) {
        sacc = mfma32(fr[s], qf[s % 12], sacc);
        if (s + 12 < 32) qf[s % 12] = *(const LAS v4u*)QADDR(s + 12);
        SB();
      }
      float mx = fmaxf(fmaxf(sacc[0], sacc[1]), sacc[2]);
      PIN(mx); SB();
      mx = max3(max3(mx, sacc[3], sacc[4]), max3(sacc[5], sacc[6], sacc[7]), max3(max3(sacc[8], sacc[9], sacc[10]), max3(sacc[11], sacc[12], sacc[13]), max3(sacc[14], sacc[15], mx)));
      *(LAS float*)(L + mxw) = mx;
      LBAR();
      STAMP(6);
      const f4 m0 = *(const LAS f4*)(L + L_MX + (l & 31) * 32), m1 = *(const LAS f4*)(L + L_MX + (l & 31) * 32 + 16);
      mx = max3(max3(m0.x, m0.y, m0.z), max3(m0.w, m1.x, m1.y), max3(m1.z, m1.w, m1.w)) * SCL;
      float e[16];
#pragma unroll
      for (int i = 0; i < 16; ++i) e[i] = __builtin_amdgcn_exp2f(__builtin_fmaf(sacc[i], SCL, -mx));
      *(LAS v4u*)(L + L_P + ((2 * w) * 64 + l) * 16) = v4u{pk2(e[0], e[1]), pk2(e[2], e[3]), pk2(e[4], e[5]), pk2(e[6], e[7])};
      *(LAS v4u*)(L + L_P + ((2 * w + 1) * 64 + l) * 16) = v4u{pk2(e[8], e[9]), pk2(e[10], e[11]), pk2(e[12], e[13]), pk2(e[14], e[15])};
      float sum = (((e[0] + e[1]) + (e[2] + e[3])) + ((e[4] + e[5]) + (e[6] + e[7]))) + (((e[8] + e[9]) + (e[10] + e[11])) + ((e[12] + e[13]) + (e[14] + e[15])));
      *(LAS float*)(L + mxw + L_LS) = sum;
      LBAR();
      STAMP(7);
      if (w == 0 && l < 32) {
        const f4 s0 = *(const LAS f4*)(L + L_LS + l * 32), s1 = *(const LAS f4*)(L + L_LS + l * 32 + 16);
        sum = ((s0.x + s0.y) + (s0.z + s0.w)) + ((s1.x + s1.y) + (s1.z + s1.w));
        sth(v4u{__float_as_uint(mx), e1, __float_as_uint(sum), e1}, rw, W_ML + bk * 512 + l * 16);
      }
      if (KNOB(2) == 5 && w == 1 && l == 0) sth2(mktag(e1), rw, W_FLAG + (64 + bk) * 128);
      RBAR();
      v4u rb[4];
#pragma unroll
      for (int n = 0; n < 4; ++n) rb[n] = *(const LAS v4u*)(L + sr_ + n * 8 * 272);
#pragma unroll
      for (int n = 0; n < 4; ++n) sths(rb[n], rw, oo, n * 8192);
      RBAR();
    } else {
      const int w4 = w - 4;
      e1 = __builtin_amdgcn_readfirstlane(ep) + 1u;
      asm volatile("" :: "v"(cpf));
      const int bk2 = (l >> 4) & 1, q = (l & 15) >> 2, pp = l & 3;
#define VFRAGS() do { \
      _Pragma("unroll") for (int ct = 0; ct < 4; ++ct) _Pragma("unroll") for (int kk = 0; kk < 8; ++kk) { \
          const int r0 = 16 * kk + 4 * hh + q, r1 = r0 + 8, ch = 4 * ct + 2 * bk2 + (pp >> 1); \
          LAS unsigned char* base = L + w4 * 32768 + 8 * (pp & 1); \
          fr[ct * 8 + kk] = trfrag(base + r0 * 256 + 16 * (ch ^ swz(r0)), base + r1 * 256 + 16 * (ch ^ swz(r1))); } \
      _Pragma("unroll") for (int s = 0; s < 32; ++s) PIN(fr[s]); } while (0)
      const v2u tag = mktag(e1);
      v2u fl = tag;
      const int nb = w4 < 2 ? 1 : w4;
#pragma unroll 1
      for (int k = 0; k < nb; ++k) RBAR();
      if (w4 == 3) { STAMP(5); if (KNOB(2) != 5) fl = ldh2(rw, W_FLAG + (64 + bk) * 128); }
      SB();
      VFRAGS();
      if (w4 == 3 && __any((fl.x != tag.x) | (fl.y != tag.y))) wait_flags(rw, 64 + bk, tag);
      STAMP(3);
      asm volatile("s_waitcnt lgkmcnt(0)" ::: "memory");
#pragma unroll 1
      for (int k = nb; k < 3; ++k) RBAR();
      int sw_ = L_SL + w4 * 8704 + (l & 31) * 272 + 8 * hh;
      PIN(sw_); PIN(sr_); PIN(oo);
      if (KNOB(2) == 5) {
        if (w4 < 3) {
#pragma unroll
          for (int i = 0; i < 14; ++i) {
            const int u = w4 * 14 + i;
            sths(sent4, rw, l * 16, u < 32 ? W_OP + bk * 32768 + u * 1024 : W_PO + bk * 10240 + (u - 32) * 1024);
          }
        }
        STAMP(1);
      }
      RBAR();
      if (KNOB(2) == 5) { DRAIN(); STAMP(2); }
      RBAR();
      STAMP(7);
      v4u pf[8];
#pragma unroll
      for (int kk = 0; kk < 8; ++kk) pf[kk] = *(const LAS v4u*)(L + L_P + (kk * 64 + l) * 16);
      SB();
      f16v o[4];
#pragma unroll
      for (int ct = 0; ct < 5; ++ct) {
        if (ct < 4) {
#pragma unroll
          for (int i = 0; i < 16; ++i) o[ct][i] = 0.f;
        }
#pragma unroll
        for (int kk = 0; kk < 8; ++kk) {
          if (ct < 4) o[ct] = mfma32(fr[ct * 8 + kk], pf[kk], o[ct]);
          SB();
          if (ct > 0 && kk >= 1 && kk < 5) {
            const int g4 = kk - 1, pt = ct - 1;
            *(LAS v2u*)(L + sw_ + (32 * pt + 8 * g4) * 2) = v2u{guard(pk2(o[pt][4 * g4], o[pt][4 * g4 + 1])), guard(pk2(o[pt][4 * g4 + 2], o[pt][4 * g4 + 3]))};
          }
          if (ct == 2 && kk == 6) LBAR();
          SB();
        }
      }
      STAMP(6);
      LBAR();
    }
    {
      const int half = (w >> 2) * 2;
      const v4u r0 = *(const LAS v4u*)(L + sr_ + half * 8 * 272 + 128), r1 = *(const LAS v4u*)(L + sr_ + (half + 1) * 8 * 272 + 128);
      sth(r0, rw, oo + half * 8192 + 128);
      sth(r1, rw, oo + half * 8192 + 8192 + 128);
    }
    STAMP(8);
    if (i2 < 40) {
      const int du = i2, db = du / 20;
      float gate;
      {
        const rsrc_t rg = mk(p.gw, M * 2);
        float s = 0.f;
#pragma unroll
        for (int i = 0; i < 5; ++i) {
          const u32 xa = __builtin_amdgcn_raw_buffer_load_b32(rx, (db * M + 128 * i + 2 * l) * 2, 0, 0);
          const u32 ga = __builtin_amdgcn_raw_buffer_load_b32(rg, (128 * i + 2 * l) * 2, 0, 0);
          s += bflo(xa) * bflo(ga) + bfhi(xa) * bfhi(ga);
        }
        s += rorn<1>(s); s += rorn<2>(s); s += rorn<4>(s); s += rorn<8>(s);
        s = swap16add(s, s); s = swap32add(s, s);
        gate = 1.f / (1.f + __expf(-s));
      }
      const int pr = tid >> 3;
      wait_flags(rw, 64 + 4 * w + ((l >> 3) & 3) + 32 * (l >> 5), mktag(e1));
      int lw = ((4 * (l & 7) + g) * 8 + w) * 4;
      PIN(lw);
      LAS unsigned char* LD = L + 65536;
      STAMP(9);
      f4 v;
      {
        v4u t0, t1, f0, f1;
        const int o = W_PO + pr * 5120 + du * 128 + (l & 7) * 16;
        const int of = W_PF + (int)umin(pr, 53) * 10240 + du * 256 + (l & 7) * 32;
        bool fok = false;
#pragma unroll 1
        for (u32 sp = 0; sp < SPIN_CAP; ++sp) {
          CBAR();
          t0 = ldh(rw, o); t1 = ldhs(rw, o, 64 * 5120);
          if (!fok) {
            f0 = ldh(rw, of); f1 = ldh(rw, of + 16);
            fok = !__any((f0.y != e1) | (f0.w != e1) | (f1.y != e1) | (f1.w != e1));
          }
          if (fok && !__any(umax(max4(t0), max4(t1)) == SENT)) break;
        }
        const float gf = pr < 54 ? 1.f - gate : 0.f;
        v = f4{__uint_as_float(f0.x), __uint_as_float(f0.z), __uint_as_float(f1.x), __uint_as_float(f1.z)} * gf + (__builtin_bit_cast(f4, t0) + __builtin_bit_cast(f4, t1)) * gate;
      }
      STAMP(10);
      float s = swap32add(swap16add(v.x, v.y), swap16add(v.z, v.w));
      s += rorn<8>(s);
      *(LAS float*)(LD + lw) = s;
      LBAR();
      if (w == 0) {
        const f4 s0 = *(const LAS f4*)(LD + (l & 31) * 32), s1 = *(const LAS f4*)(LD + (l & 31) * 32 + 16);
        const float t = ((s0.x + s0.y) + (s0.z + s0.w)) + ((s1.x + s1.y) + (s1.z + s1.w));
        if (l < 32) p.out[32 * du + l] = (u16)pk2(t, t);
        if (du == 0 && l == 0) sth1(e1, rw, W_EPOCH);
        if (du == 0) {
          const u64 now = __builtin_amdgcn_s_memrealtime();
          u64 mn = ~0ull, mx = 0;
          for (int i = 0; i < 4; ++i) { const u64 t = __hip_atomic_load((GAS u64*)(p.ws + W_DBG) + i * 64 + l, __ATOMIC_RELAXED, __HIP_MEMORY_SCOPE_AGENT); mn = t < mn ? t : mn; }
          for (int i = 0; i < 32; ++i) { const u64 t = __hip_atomic_load((GAS u64*)(p.ws + W_DBG) + 256 + i * 64 + l, __ATOMIC_RELAXED, __HIP_MEMORY_SCOPE_AGENT); mx = t > mx ? t : mx; }
          for (int o = 1; o < 64; o <<= 1) { const u64 a = __shfl_xor(mn, o), b2 = __shfl_xor(mx, o); mn = a < mn ? a : mn; mx = b2 > mx ? b2 : mx; }
          u64 extra = 300 + (99 == 99 ? 0 : 99 == 98 ? 2 * (now - mn) : 2 * (mx - mn));
          if (extra > 10000) extra = 10000;
          while (__builtin_amdgcn_s_memrealtime() < now + extra) {}
        }
      }
      STAMP(11);
    }
    goto done;
  }
  if (bid >= 128 || j < 2) {
    const int ac = h * 4 + j;
    HOLD(3);
    const int cj = j >> 1, dj = j & 1;
    {
      const u16* sv = p.wuv + ((size_t)(256 * cj + 32 * w + (l >> 3)) * H + h) * D + 64 * dj;
#pragma unroll
      for (int n = 0; n < 4; ++n) {
        const int fz = 2 * (((l >> 4) & 1) | ((n & 1) << 1));
        if (n < KNOB(10) && bid < 128) THR(7);
        dma(sv + n * 8 * H * D + 8 * ((l & 7) ^ fz), L + (w * 4 + n) * 1024);
      }
      const u16* so = p.wo + ((size_t)h * D + 64 * dj + 8 * w + g) * M;
      const int fo = 2 * (g | ((w & 1) << 2));
#pragma unroll
      for (int n = 0; n < 10; ++n) { if (n + 4 < KNOB(10) && bid < 128) THR(7); dma(so + (n & 1) * 4 * M + (n >> 1) * 128 + 8 * (r ^ fo), L + 32768 + (n >> 1) * 16384 + (8 * w + 4 * (n & 1)) * 256); }
    }
    const int cb_b = w >> 2, cb = (w >> 1) & 1, kh = w & 1;
    constexpr int L_A2 = 114688, L_A3 = L_A2 + 4 * 528, L_WT = 118784;
    int a2w = L_A2 + (cb_b * 2 + kh) * 528 + (128 * cb + 8 * r + 4 * (g & 1)) * 2;
    int a2r = L_A2 + (r & 3) * 528 + 16 * g;
    int a3w = L_A3 + (g & 1) * 144 + (16 * (w & 3) + r) * 2;
    int a3r = L_A3 + (r >> 3) * 144 + 16 * g;
    int po = W_PO + ac * 5120 + ((g >> 1) * M + 32 * w + 16 * (g & 1) + r) * 4;
    int oo = W_OP + (((cb_b * 32 + 16 * kh + g) * H + h) * DC + 256 * cj + 128 * cb + 8 * r) * 2;
    PIN(a2w); PIN(a2r); PIN(a3w); PIN(a3r); PIN(po); PIN(oo);
    if (KNOB(2) != 5) wait_flags(rw, 64 + 8 * w + (l >> 3), mktag(e1));
    DRAIN();
    RBAR();
    STAMP(6);
    v4u of[4], ml = {0u, 0u, 0u, 0u};
    if (w == 7) ml = ldh(rw, W_ML + (l * H + h) * 16);
#pragma unroll
    for (int i = 0; i < 4; ++i) of[i] = KNOB(2) == 5 ? sent4 : ldhs(rw, oo, i * 4 * H * DC * 2);
    SB();
    v4u wv[8], wo[12];
    {
      const int q = r >> 2, pp = l & 3;
#pragma unroll
      for (int ks = 0; ks < 8; ++ks) {
        const int row = 32 * ks + 8 * g + q, fz = 2 * (((q >> 1) & 1) | ((g & 1) << 1));
        LAS unsigned char* a = L + row * 128 + 16 * ((2 * (w & 3) + (pp >> 1)) ^ fz) + 8 * (pp & 1);
        wv[ks] = trfrag(a, a + 512);
      }
#pragma unroll
      for (int pi = 0; pi < 3; ++pi) {
        const int P = (pi == 2 ? (w & 3) : w) + 8 * pi;
#pragma unroll
        for (int tt = 0; tt < 2; ++tt)
#pragma unroll
          for (int ks = 0; ks < 2; ++ks) {
            const int row = 32 * ks + 8 * g + q, fo = 2 * (q | ((g & 1) << 2));
            LAS unsigned char* a = L + 32768 + (P >> 2) * 16384 + row * 256 + 16 * ((4 * (P & 3) + 2 * tt + (pp >> 1)) ^ fo) + 8 * (pp & 1);
            wo[pi * 4 + tt * 2 + ks] = trfrag(a, a + 1024);
          }
      }
#pragma unroll
      for (int i = 0; i < 8; ++i) PIN(wv[i]);
#pragma unroll
      for (int i = 0; i < 12; ++i) PIN(wo[i]);
    }
    STAMP(11);
    if (w == 7) {
#pragma unroll 1
      for (u32 sp = 0; sp < SPIN_CAP; ++sp) {
        if (!__any((ml.y != e1) | (ml.w != e1))) break;
        CBAR();
        ml = ldh(rw, W_ML + (l * H + h) * 16);
      }
      STAMP(10);
      const float mm = __uint_as_float(ml.x), ll = __uint_as_float(ml.z);
      float mx = mm;
      mx = fmaxf(mx, rorn<1>(mx)); mx = fmaxf(mx, rorn<2>(mx)); mx = fmaxf(mx, rorn<4>(mx)); mx = fmaxf(mx, rorn<8>(mx));
      { auto t = __builtin_amdgcn_permlane16_swap(__float_as_uint(mx), __float_as_uint(mx), false, false); mx = fmaxf(__uint_as_float(t[0]), __uint_as_float(t[1])); }
      const float e = __builtin_amdgcn_exp2f(mm - mx);
      float sum = e * ll;
      sum += rorn<1>(sum); sum += rorn<2>(sum); sum += rorn<4>(sum); sum += rorn<8>(sum);
      sum = swap16add(sum, sum);
      const float wgt = e * __builtin_amdgcn_rcpf(sum);
      *(LAS u32*)(L + L_WT + ((l & 48) + (l & 3) * 4 + ((l >> 2) & 3)) * 4) = pk2(wgt, wgt);
    }
    LBAR();
    STAMP(7);
    v4u wsel[4];
    {
      const v4u wt = *(const LAS v4u*)(L + L_WT + (cb_b * 32 + kh * 16 + g * 4) * 4);
      bool bad = __any(umax(umax(max4(of[0]), max4(of[1])), umax(max4(of[2]), max4(of[3]))) == SENT);
      if (bad) {
#pragma unroll
        for (int i = 0; i < 4; ++i) of[i] = ldhs(rw, oo, i * 4 * H * DC * 2);
      }
      SB();
      wsel[0] = selfrag(wt.x, mask); wsel[1] = selfrag(wt.y, mask); wsel[2] = selfrag(wt.z, mask); wsel[3] = selfrag(wt.w, mask);
#pragma unroll
      for (int i = 0; i < 4; ++i) PIN(wsel[i]);
      SB();
#pragma unroll 1
      for (u32 sp = 0; sp < SPIN_CAP; ++sp) {
        u32 bd = 0;
#pragma unroll
        for (int i = 0; i < 4; ++i) bd |= __any(max4(of[i]) == SENT) ? 1u << i : 0u;
        if (!bd) break;
        CBAR(); SB();
#pragma unroll
        for (int i = 0; i < 4; ++i) if (bd >> i & 1) of[i] = ldhs(rw, oo, i * 4 * H * DC * 2);
      }
    }
    STAMP(8);
    {
      f4 acc = {0.f, 0.f, 0.f, 0.f};
#pragma unroll
      for (int i = 0; i < 4; ++i) acc = mfma16(wsel[i], of[i], acc);
      *(LAS v2u*)(L + a2w) = v2u{pk2(acc.x, acc.y), pk2(acc.z, acc.w)};
    }
    LBAR();
    if (w < 4) {
      v4u af[8];
#pragma unroll
      for (int ks = 0; ks < 8; ++ks) af[ks] = *(const LAS v4u*)(L + a2r + 64 * ks);
      SB();
      f4 acc = {0.f, 0.f, 0.f, 0.f};
#pragma unroll
      for (int ks = 0; ks < 8; ++ks) acc = mfma16(af[ks], wv[ks], acc);
      const float v = (g & 1) ? acc.z + acc.w : acc.x + acc.y;
      *(LAS u16*)(L + a3w) = (u16)pk2(v, v);
    }
    LBAR();
    {
      const v4u a0 = *(const LAS v4u*)(L + a3r), a1 = *(const LAS v4u*)(L + a3r + 64);
#pragma unroll
      for (int pi = 0; pi < 3; ++pi) {
        if (pi == 2 && w >= 4) break;
        f4 t0 = {0.f, 0.f, 0.f, 0.f}, t1 = {0.f, 0.f, 0.f, 0.f};
        t0 = mfma16(a0, wo[pi * 4 + 0], t0); t1 = mfma16(a0, wo[pi * 4 + 2], t1);
        t0 = mfma16(a1, wo[pi * 4 + 1], t0); t1 = mfma16(a1, wo[pi * 4 + 3], t1);
        sth1(guard(__float_as_uint((g & 1) ? t1.x : t0.x)), rw, po + pi * 1024);
      }
    }
    STAMP(9);
  } else {
    const int i3 = h * 2 + j - 2;
    HOLD(4);
    if (i3 < 54) {
      const int pair = i3 >> 1, half = i3 & 1;
      STAMP(6);
      u32 xv[5];
#pragma unroll
      for (int f = 0; f < 5; ++f) xv[f] = __builtin_amdgcn_raw_buffer_load_b32(rx, (bsel * M + 320 * half + 40 * w + 10 * g) * 2 + 4 * f, 0, 0);
      v4u wf[10], wdn[8];
      const GAS u16* src = (const GAS u16*)((r >> 3) ? p.wu : p.wg) + (320 * half + 40 * w + 10 * g) * F + 64 * pair + 8 * (r & 7);
      if (w >= 1 && w < 6) {
        const rsrc_t rd = mk(p.wd, F * M * 2);
        const int vo = ((32 * i3 + 8 * g) * M + 128 * (w - 1) + 8 * r) * 2;
#pragma unroll
        for (int f = 0; f < 8; ++f) { if (f < KNOB(10)) THR(8); wdn[f] = ldw(rd, vo, f * M * 2); }
      } else {
#pragma unroll
        for (int f = 0; f < 8; ++f) wdn[f] = v4u{0u, 0u, 0u, 0u};
      }
      SB();
#pragma unroll
      for (int f = 0; f < 10; ++f) { if (f < KNOB(10) && (w == 0 || w >= 6)) THR(8); wf[f] = __builtin_nontemporal_load((const GAS v4u*)(src + f * F)); SB(); }
      f4 acc = {0.f, 0.f, 0.f, 0.f};
#pragma unroll
      for (int f = 0; f < 5; ++f) {
        acc = mfma16(selfrag(duplo(xv[f]), mask), wf[2 * f], acc);
        acc = mfma16(selfrag(duphi(xv[f]), mask), wf[2 * f + 1], acc);
      }
      *(LAS f4*)(L + (w * 64 + l) * 16) = acc;
      STAMP(7);
      LBAR();
      if (w == 0) {
        f4 s = *(const LAS f4*)(L + l * 16);
#pragma unroll
        for (int i = 1; i < 8; ++i) s += *(const LAS f4*)(L + (i * 64 + l) * 16);
        sth(v4u{__float_as_uint(s.x), e1, __float_as_uint(s.y), e1}, rw, W_FX + i3 * 2048 + l * 16);
        sth(v4u{__float_as_uint(s.z), e1, __float_as_uint(s.w), e1}, rw, W_FX + i3 * 2048 + 1024 + l * 16);
        v4u z0, z1;
#pragma unroll 1
        for (u32 sp = 0; sp < SPIN_CAP; ++sp) {
          CBAR();
          z0 = ldh(rw, W_FX + (i3 ^ 1) * 2048 + l * 16); z1 = ldh(rw, W_FX + (i3 ^ 1) * 2048 + 1024 + l * 16);
          if (!__any((z0.y != e1) | (z0.w != e1) | (z1.y != e1) | (z1.w != e1))) break;
        }
        const f4 o = {__uint_as_float(z0.x), __uint_as_float(z0.z), __uint_as_float(z1.x), __uint_as_float(z1.z)};
        s += o;
        float hv[4];
#pragma unroll
        for (int i = 0; i < 4; ++i) { const float gs = s[i], us = rorn<8>(s[i]); hv[i] = gs * __builtin_amdgcn_rcpf(1.f + __expf(-gs)) * us; }
        if ((r >> 2) == half) *(LAS v2u*)(L + 8192 + (g >> 1) * 64 + (8 * (r & 3) + 4 * (g & 1)) * 2) = v2u{pk2(hv[0], hv[1]), pk2(hv[2], hv[3])};
      }
      LBAR();
      if (w >= 1 && w < 6) {
        const v4u z = *(const LAS v4u*)(L + 8192 + bsel * 64 + g * 16);
        const u32 zz[4] = {z.x, z.y, z.z, z.w};
        acc = f4{0.f, 0.f, 0.f, 0.f};
#pragma unroll
        for (int f = 0; f < 4; ++f) {
          acc = mfma16(selfrag(duplo(zz[f]), mask), wdn[2 * f], acc);
          acc = mfma16(selfrag(duphi(zz[f]), mask), wdn[2 * f + 1], acc);
        }
        const int o = W_PF + i3 * 10240 + ((g >> 1) * M + 128 * (w - 1) + 8 * r + 4 * (g & 1)) * 8;
        sth(v4u{__float_as_uint(acc.x), e1, __float_as_uint(acc.y), e1}, rw, o);
        sth(v4u{__float_as_uint(acc.z), e1, __float_as_uint(acc.w), e1}, rw, o + 16);
      }
      STAMP(8);
    }
  }
done:;
}

extern "C" void kernel_launch(void* const* d_in, const int* in_sizes, int n_in,
                              void* d_out, int out_size, void* d_ws, size_t ws_size,
                              hipStream_t stream) {
  Params p;
  p.x = (const u16*)d_in[0]; p.ckv = (const u16*)d_in[1]; p.wq = (const u16*)d_in[2]; p.wuk = (const u16*)d_in[3];
  p.wuv = (const u16*)d_in[4]; p.wo = (const u16*)d_in[5]; p.wg = (const u16*)d_in[7]; p.wu = (const u16*)d_in[8];
  p.wd = (const u16*)d_in[9]; p.gw = (const u16*)d_in[10]; p.out = (u16*)d_out; p.ws = (unsigned char*)d_ws + WS_OFF(ws_size);
  (void)hipFuncSetAttribute((const void*)mk_kernel, hipFuncAttributeMaxDynamicSharedMemorySize, LDS_BYTES);
  mk_kernel<<<256, NT, LDS_BYTES, stream>>>(p);
}
```

```cpp
#include <hip/hip_runtime.h>
#include <stdint.h>


typedef unsigned short u16;
typedef unsigned int u32;
typedef unsigned long long u64;
typedef u32 v4u __attribute__((ext_vector_type(4)));
typedef u32 v2u __attribute__((ext_vector_type(2)));
typedef float f4 __attribute__((ext_vector_type(4)));
typedef float f2 __attribute__((ext_vector_type(2)));
typedef float f16v __attribute__((ext_vector_type(16)));
typedef short bf8 __attribute__((ext_vector_type(8)));
typedef short s4 __attribute__((ext_vector_type(4)));
typedef __bf16 bf2 __attribute__((ext_vector_type(2)));
#define GAS __attribute__((address_space(1)))
#define LAS __attribute__((address_space(3)))
#define DI __device__ __forceinline__
typedef __amdgpu_buffer_rsrc_t rsrc_t;

constexpr int NB = 2, H = 32, SK = 4096, D = 128, M = 640, DC = 512, F = 1728;
constexpr int NT = 512;
constexpr int W_EPOCH = 0;
constexpr int W_FLAG = 4096;
constexpr int W_ML = 24576;
constexpr int W_QA = 65536;
constexpr int W_FX = 131072;
constexpr int W_QP = 262144;
constexpr int W_PO = 524288;
constexpr int W_PF = 1179648;
constexpr int W_OP = 2097152;
constexpr int W_END = 4194304;
constexpr int LDS_BYTES = 163840;
constexpr int SC1 = 16, NTL = 2;
constexpr u32 SENT = 0xFFFFFFFFu;
constexpr u32 SPIN_CAP = 1u << 20;
constexpr int CODE_LINES = 17280 / 128;
constexpr int TILE_HOLD = 140;
constexpr int HELP_BLOCKS = 2;
constexpr float SCL = 0.08838834764831845f * 1.4426950408889634f;

struct Params {
  const u16* x; const u16* ckv; const u16* wq; const u16* wuk; const u16* wuv; const u16* wo;
  const u16* wg; const u16* wu; const u16* wd; const u16* gw; u16* out; unsigned char* ws;
};

DI rsrc_t mk(const void* p, int bytes) { return __builtin_amdgcn_make_buffer_rsrc((void*)p, 0, bytes, 0x00020000); }
DI v4u ldw(rsrc_t r, int voff, int soff) { return __builtin_amdgcn_raw_buffer_load_b128(r, voff, soff, NTL); }
DI v4u ldh(rsrc_t r, int voff, int soff = 0) { return __builtin_amdgcn_raw_buffer_load_b128(r, voff, soff, SC1); }
DI v2u ldh2(rsrc_t r, int voff) { return __builtin_amdgcn_raw_buffer_load_b64(r, voff, 0, SC1); }
DI void sth(v4u v, rsrc_t r, int voff, int soff = 0) { __builtin_amdgcn_raw_buffer_store_b128(v, r, voff, soff, SC1); }
DI void sth2(v2u v, rsrc_t r, int voff) { __builtin_amdgcn_raw_buffer_store_b64(v, r, voff, 0, SC1); }
DI void sth1(u32 v, rsrc_t r, int voff) { __builtin_amdgcn_raw_buffer_store_b32(v, r, voff, 0, SC1); }
DI u32 umin(u32 a, u32 b) { return a < b ? a : b; }
DI u32 umax(u32 a, u32 b) { return a > b ? a : b; }
DI u32 guard(u32 v) { return umin(v, 0xFFFFFFFEu); }
DI v4u guard4(v4u v) { return v4u{guard(v.x), guard(v.y), guard(v.z), guard(v.w)}; }
DI u32 max4(v4u v) { return umax(umax(v.x, v.y), umax(v.z, v.w)); }
DI u32 fu(float v) { return __float_as_uint(v); }
DI float uf(u32 v) { return __uint_as_float(v); }
DI u32 pk2(float lo, float hi) { bf2 r = {(__bf16)lo, (__bf16)hi}; return __builtin_bit_cast(u32, r); }
DI float bflo(u32 v) { return uf(v << 16); }
DI float bfhi(u32 v) { return uf(v & 0xFFFF0000u); }
DI f4 mfma16(v4u a, v4u b, f4 c) { return __builtin_amdgcn_mfma_f32_16x16x32_bf16(__builtin_bit_cast(bf8, a), __builtin_bit_cast(bf8, b), c, 0, 0, 0); }
DI f16v mfma32(v4u a, v4u b, f16v c) { return __builtin_amdgcn_mfma_f32_32x32x16_bf16(__builtin_bit_cast(bf8, a), __builtin_bit_cast(bf8, b), c, 0, 0, 0); }
#define CBAR() asm volatile("" ::: "memory")
#define SB() __builtin_amdgcn_sched_barrier(0)
#define PIN(x) asm volatile("" : "+v"(x))
#define DRAIN() asm volatile("s_waitcnt vmcnt(0)" ::: "memory")
#define VMWAIT(n) asm volatile("s_waitcnt vmcnt(" #n ")" ::: "memory")
#define LWAIT() asm volatile("s_waitcnt lgkmcnt(0)" ::: "memory")
#define RBAR() do { CBAR(); __builtin_amdgcn_s_barrier(); CBAR(); } while (0)
#define LBAR() do { LWAIT(); __builtin_amdgcn_s_barrier(); CBAR(); } while (0)
#define LP(T, off) ((LAS T*)(size_t)(off))
#define POLL(LOADS, BAD) do { u32 sp_ = SPIN_CAP; _Pragma("unroll 1") do { CBAR(); LOADS; } while ((BAD) && --sp_); CBAR(); } while (0)
template <int N> DI float rorn(float v) { return __builtin_bit_cast(float, __builtin_amdgcn_update_dpp(0, __builtin_bit_cast(int, v), 0x120 + N, 0xf, 0xf, false)); }
DI float max3(float a, float b, float c) { float d; asm("v_max3_f32 %0, %1, %2, %3" : "=v"(d) : "v"(a), "v"(b), "v"(c)); return d; }
DI float add1(float a, float b) { float d; asm("v_add_f32 %0, %1, %2" : "=v"(d) : "v"(a), "v"(b)); return d; }
DI float swap16add(float x, float y) { auto t = __builtin_amdgcn_permlane16_swap(fu(x), fu(y), false, false); return uf(t[0]) + uf(t[1]); }
DI float swap32add(float x, float y) { auto t = __builtin_amdgcn_permlane32_swap(fu(x), fu(y), false, false); return uf(t[0]) + uf(t[1]); }
DI v4u selfrag(u32 vv, v4u mask) { return v4u{vv & mask.x, vv & mask.y, vv & mask.z, vv & mask.w}; }
DI u32 duplo(u32 v) { return __builtin_amdgcn_perm(v, v, 0x01000100u); }
DI u32 duphi(u32 v) { return __builtin_amdgcn_perm(v, v, 0x03020302u); }
DI int swz(int row) { return ((row & 3) << 2) | ((row >> 2) & 3); }
DI void dma(const u16* src, int lds) { __builtin_amdgcn_global_load_lds((const GAS u32*)src, LP(u32, lds), 16, 0, NTL); }
DI void dma_asm(const u16* src, u32 lds) {
  u32 keep;
  asm volatile("s_mov_b32 %0, m0\n\ts_mov_b32 m0, %2\n\ts_nop 0\n\tglobal_load_lds_dwordx4 %1, off nt\n\ts_mov_b32 m0, %0" : "=&s"(keep) : "v"(src), "s"(lds) : "memory");
}
DI v4u trfrag(LAS unsigned char* a0, LAS unsigned char* a1) {
  const v2u lo = __builtin_bit_cast(v2u, __builtin_amdgcn_ds_read_tr16_b64_v4i16((LAS s4*)a0));
  const v2u hi = __builtin_bit_cast(v2u, __builtin_amdgcn_ds_read_tr16_b64_v4i16((LAS s4*)a1));
  return v4u{lo.x, lo.y, hi.x, hi.y};
}
template <int CT, int KK> DI void pv_epi(int addr, const f16v& o) {
  u32 t0, t1;
  asm volatile("v_cvt_pk_bf16_f32 %0, %2, %3\n\tv_cvt_pk_bf16_f32 %1, %4, %5"
               : "=&v"(t0), "=&v"(t1) : "v"(o[4 * KK]), "v"(o[4 * KK + 1]), "v"(o[4 * KK + 2]), "v"(o[4 * KK + 3]), "v"(o));
  *LP(v2u, addr ^ ((4 * CT + KK) << 4)) = v2u{t0, t1};
}
DI u32 tag2(u32 e1) { return e1 * 0x9E3779B1u + 0x5bd1e995u; }
DI v4u gran(u32 a, u32 b, u32 e1) { return v4u{a, e1, b, tag2(e1)}; }
DI bool stale(v4u v, u32 e1) { return (v.y != e1) | (v.w != tag2(e1)); }
DI bool stale2(v2u v, u32 e1) { return (v.x != e1) | (v.y != tag2(e1)); }
#define KEY4(ks, i) (128 * (i) + 4 * (((ks) + 11 * (i)) & 31))


DI void wait_flags(rsrc_t rw, int idx, u32 e1) {
#pragma unroll 1
  for (u32 sp = 0; sp < SPIN_CAP; ++sp) {
    CBAR();
    if (!__any(stale2(ldh2(rw, W_FLAG + idx * 128), e1))) break;
    __builtin_amdgcn_s_sleep(1);
  }
  CBAR();
}

__global__ void __launch_bounds__(NT, 2) mk_kernel(Params p) {
  extern __shared__ __attribute__((aligned(16))) unsigned char smem[];
  LAS unsigned char* L = (LAS unsigned char*)smem;
  const int tid = threadIdx.x, l = tid & 63, w = __builtin_amdgcn_readfirstlane(tid >> 6);
  const int bid = blockIdx.x;
  const int r = l & 15, g = l >> 4;
  const u64 t_start = __builtin_amdgcn_s_memrealtime();
  u64 pc0; asm volatile("s_getpc_b64 %0" : "=s"(pc0));
  const GAS u32* code = (const GAS u32*)(pc0 & ~127ull);
  u32 cpf = 0;
  if (w >= 5) cpf = code[umin(tid - 320, CODE_LINES - 1) * 32];
  asm volatile("" :: "s"(p.x), "s"(p.ws) : "memory");
  SB();
  if (__builtin_expect(bid >= 192 && w < 4, 1)) {
    const u16* src = p.wuk + ((size_t)(256 * ((bid >> 3) & 1) + 64 * w + 16 * g) * H + (bid & 7) * 4 + ((bid - 192) >> 4)) * D;
#pragma unroll 1
    for (int n = 0; n < 16; ++n) dma_asm(src + n * H * D + 8 * (r ^ ((8 * g + (n >> 1)) & 15)), w * 16384 + n * 1024);
  }
  SB();
  const rsrc_t rw = mk(p.ws, W_END);
  u32 ep = 0;
  if (!(bid < 128 && w >= 2)) ep = __builtin_amdgcn_raw_buffer_load_b32(rw, W_EPOCH, 0, SC1);
  SB();
  const rsrc_t rx = mk(p.x, NB * M * 2);
  const u32 m16 = (l & 1) ? 0xFFFF0000u : 0x0000FFFFu;
  const int jq = (l & 7) >> 1;
  const v4u mask = {jq == 0 ? m16 : 0u, jq == 1 ? m16 : 0u, jq == 2 ? m16 : 0u, jq == 3 ? m16 : 0u};
  const int bsel = (l >> 3) & 1;
  const v4u sent4 = {SENT, SENT, SENT, SENT};
  int h, j;
  u32 e1;

  if (__builtin_expect(bid < 128, 1)) {
    h = (bid & 7) * 4 + (bid >> 5); j = (bid >> 3) & 3;
    if (__builtin_expect(w < 2, 1)) {
      const rsrc_t rq = mk(p.wq, M * H * D * 2);
      const int mb = 160 * j + 80 * w + 20 * g;
      u32 xv[10];
#pragma unroll
      for (int f = 0; f < 10; ++f) xv[f] = __builtin_amdgcn_raw_buffer_load_b32(rx, (bsel * M + mb) * 2 + 4 * f, 0, 0);
      v4u wa[20];
      const int vo = (mb * (H * D) + h * D + 8 * r) * 2;
#pragma unroll
      for (int f = 0; f < 20; ++f) { wa[f] = ldw(rq, vo, f * H * D * 2); SB(); }
      v4u sel[20];
#pragma unroll
      for (int f = 0; f < 10; ++f) { sel[2 * f] = selfrag(duplo(xv[f]), mask); sel[2 * f + 1] = selfrag(duphi(xv[f]), mask); }
#pragma unroll
      for (int f = 0; f < 20; ++f) PIN(sel[f]);
      e1 = __builtin_amdgcn_readfirstlane(ep) + 1u;
      *LP(u32, 131064) = e1;
      int o = W_QP + ((h * 4 + j) * 2 + w) * 1024 + l * 16;
      PIN(o);
      SB();
      f4 acc = {0.f, 0.f, 0.f, 0.f};
#pragma unroll
      for (int f = 0; f < 20; ++f) acc = mfma16(sel[f], wa[f], acc);
      sth(gran(pk2(acc.x, acc.y), pk2(acc.z, acc.w), e1), rw, o);
    } else {
      asm volatile("" :: "v"(cpf));
    }
    LBAR();
    e1 = __builtin_amdgcn_readfirstlane(*LP(const u32, 131064));
    if (j < 2) {
      const int i2 = (bid & 15) + 16 * (bid >> 5), b = (i2 & 7) >> 2, ks = (i2 & 3) + 4 * (i2 >> 3);
      const u16* src = p.ckv + ((size_t)b * SK + g) * DC + 8 * r;
#pragma unroll
      for (int blk = 4 - HELP_BLOCKS; blk < 4; ++blk) {
#pragma unroll
        for (int n = 0; n < 4; ++n) dma(src + 128 * blk + KEY4(ks, 4 * w + n) * DC, 131072 + w * 4096 + n * 1024);
        RBAR();
      }
    }
  } else if (bid >= 192) {
    const int y = bid - 192, jj = (y >> 3) & 1;
    h = (y & 7) * 4 + (y >> 4);
    j = 2 + jj;
    if (w >= 4) __builtin_amdgcn_s_setprio(3);
    if (w == 4) sth(sent4, rw, W_QA + (((l >> 5) * H + h) * DC + 256 * jj) * 2 + (l & 31) * 16);
    DRAIN();
    e1 = __builtin_amdgcn_readfirstlane(ep) + 1u;
    if (w == 4 && l == 0) sth2(v2u{e1, tag2(e1)}, rw, W_FLAG + (h * 2 + jj) * 128);
    __builtin_amdgcn_s_setprio(0);
    asm volatile("" :: "v"(cpf));
    int oa = W_QA + (((g >> 1) * H + h) * DC + 256 * jj + 64 * w + 32 * (g & 1) + 2 * r) * 2;
    int qw = 65536 + ((g >> 1) * 8 + w) * 272 + (8 * r + 4 * (g & 1)) * 2, qr = 65536 + r * 272 + 16 * g;
    int o = W_QP + (h * 8 + w) * 1024 + l * 16;
    PIN(oa); PIN(qw); PIN(qr); PIN(o);
    RBAR();
    v4u u = ldh(rw, o), wk[16];
    SB();
#pragma unroll
    for (int t = 0; t < 4; ++t)
#pragma unroll
      for (int s = 0; s < 4; ++s) wk[t * 4 + s] = *(const LAS v4u*)(L + (w & 3) * 16384 + ((2 * r + (t & 1)) & 15) * 1024 + (2 * (t >> 1) + (r >> 3)) * 256 + 16 * ((4 * s + g) ^ r));
    LWAIT();
#pragma unroll
    for (int i = 0; i < 16; ++i) PIN(wk[i]);
    if (__any(stale(u, e1))) POLL(u = ldh(rw, o), __any(stale(u, e1)));
    *LP(v2u, qw) = v2u{u.x, u.z};
    LBAR();
    if (w < 4) {
      v4u qf[4];
#pragma unroll
      for (int s = 0; s < 4; ++s) qf[s] = *LP(const v4u, qr + 64 * s);
      f4 a[4];
#pragma unroll
      for (int t = 0; t < 4; ++t) a[t] = f4{0.f, 0.f, 0.f, 0.f};
#pragma unroll
      for (int s = 0; s < 4; ++s)
#pragma unroll
        for (int t = 0; t < 4; ++t) a[t] = mfma16(qf[s], wk[t * 4 + s], a[t]);
      float st[4];
#pragma unroll
      for (int t = 0; t < 4; ++t) st[t] = (a[t].x + a[t].y) + (a[t].z + a[t].w);
      sth1(guard(pk2(swap16add(st[0], st[2]), swap16add(st[1], st[3]))), rw, oa);
    }
    RBAR();
  } else {
    const int i2 = bid - 128;
    const int b = (i2 & 7) >> 2, ks = (i2 & 3) + 4 * (i2 >> 3), bk = b * 32 + ks;
    const int hh = l >> 5;
    constexpr int L_Q = 131072, L_MX = 0, L_LS = 1024, L_P = 2048, L_HF = 10240, L_SL = 16384;
    e1 = __builtin_amdgcn_readfirstlane(ep) + 1u;
    asm volatile("" : "+s"(e1) : "v"(cpf));
    v4u fr[32], qv[4];
    int sr_ = L_SL + (w & 3) * 8192 + (l >> 3) * 256 + 16 * ((l & 7) ^ (l >> 3));
    int oo = W_OP + bk * 32768 + (l >> 3) * 1024 + (128 * (w & 3) + 8 * (l & 7)) * 2;
    int sr2 = sr_ + (w >> 2) * 4096, oo2 = oo + (w >> 2) * 16384 + 128;
    int qo = W_QA + (b * 32 + w * 4) * 1024 + l * 16, qw[4];
#pragma unroll
    for (int i = 0; i < 4; ++i) { qw[i] = L_Q + (4 * w + i) * 1024 + 16 * (l ^ ((4 * w + i) & 15)); PIN(qw[i]); }
    PIN(sr2); PIN(oo2); PIN(qo);
#define QLOAD() _Pragma("unroll") for (int i = 0; i < 4; ++i) qv[i] = ldh(rw, qo, i * 1024)
#define QBAD() __any(umax(umax(max4(qv[0]), max4(qv[1])), umax(max4(qv[2]), max4(qv[3]))) == SENT)
#define QGATHER() do { if (QBAD()) POLL(QLOAD(), QBAD()); _Pragma("unroll") for (int i = 0; i < 4; ++i) *LP(v4u, qw[i]) = qv[i]; } while (0)
    if (__builtin_expect(w < 4, 1)) {
      const u16* src = p.ckv + ((size_t)b * SK + g) * DC;
      while (__builtin_amdgcn_s_memrealtime() - t_start < (u64)TILE_HOLD) __builtin_amdgcn_s_sleep(1);
#define TILE_DMA(lo, hi) _Pragma("unroll 1") for (int n = lo; n < hi; ++n) \
        dma_asm(src + KEY4(ks, 8 * w + (n & 7)) * DC + 128 * (n >> 3) + 8 * (r ^ ((g << 2) | (n & 3))), (n >> 3) * 32768 + (32 * w + 4 * (n & 7)) * 256)
      TILE_DMA(0, 24);
      v2u fl;
      asm volatile("buffer_load_dwordx2 %0, %1, %2, 0 offen sc1" : "=v"(fl) : "v"(W_FLAG + (16 * w + (l >> 2)) * 128), "s"(rw) : "memory");
      TILE_DMA(24, 32);
      const int kb = (32 * w + (l & 31)) * 256, sw = swz(l & 31);
      int qa[8];
#pragma unroll
      for (int i = 0; i < 8; ++i) { qa[i] = L_Q + (l & 31) * 1024 + 16 * ((2 * i) ^ (l & 15) ^ hh); PIN(qa[i]); }
      int mxw = L_MX + ((l & 31) * 8 + w * 2 + hh) * 4, mxr = L_MX + (l & 31) * 32, pw = L_P + (2 * w * 64 + l) * 16;
      int mlo = tid < 32 ? W_ML + bk * 512 + l * 16 : 0x40000000;
      PIN(mxw); PIN(mxr); PIN(pw); PIN(sr_); PIN(oo); PIN(mlo);
#define KFRAGS(lo, hi) _Pragma("unroll") for (int s = lo; s < hi; ++s) fr[s] = *(const LAS v4u*)(L + (s >> 3) * 32768 + kb + 16 * ((2 * (s & 7) + hh) ^ sw))
      VMWAIT(17);
      RBAR();
      KFRAGS(0, 16);
      SB();
      asm volatile("s_waitcnt vmcnt(8)" : "+v"(fl) :: "memory");
      if (__any(stale2(fl, e1))) wait_flags(rw, 16 * w + (l >> 2), e1);
      RBAR();
      QLOAD();
      SB();
      KFRAGS(16, 24);
      SB();
      VMWAIT(4);
      KFRAGS(24, 32);
      LWAIT();
#pragma unroll
      for (int s = 0; s < 32; ++s) PIN(fr[s]);
      SB();
      QGATHER();
      LBAR();
      f16v sacc;
#pragma unroll
      for (int i = 0; i < 16; ++i) sacc[i] = 0.f;
      v4u qf[12];
#pragma unroll
      for (int s = 0; s < 12; ++s) { qf[s] = *LP(const v4u, qa[s & 7] + (s >> 3) * 256); SB(); }
#pragma unroll
      for (int s = 0; s < 32; ++s) {
        sacc = mfma32(fr[s], qf[s % 12], sacc);
        if (s + 12 < 32) qf[s % 12] = *LP(const v4u, qa[(s + 12) & 7] + ((s + 12) >> 3) * 256);
        SB();
      }
      float mx = fmaxf(fmaxf(sacc[0], sacc[1]), sacc[2]);
      PIN(mx); PIN(sacc); SB();
      mx = max3(max3(mx, sacc[3], sacc[4]), max3(sacc[5], sacc[6], sacc[7]), max3(max3(sacc[8], sacc[9], sacc[10]), max3(sacc[11], sacc[12], sacc[13]), max3(sacc[14], sacc[15], mx)));
      *LP(float, mxw) = mx;
      *LP(volatile u32, L_HF + w * 4) = 0u;
      SB();
      float ml = mx * SCL;
      PIN(ml);
      float e[16];
#pragma unroll
      for (int i = 0; i < 16; ++i) e[i] = __builtin_fmaf(sacc[i], SCL, -ml);
#pragma unroll
      for (int i = 0; i < 4; ++i) e[i] = __builtin_amdgcn_exp2f(e[i]);
#pragma unroll
      for (int i = 0; i < 16; ++i) PIN(e[i]);
      SB();
      LBAR();
      const f4 m0 = *LP(const f4, mxr), m1 = *LP(const f4, mxr + 16);
      SB();
#pragma unroll
      for (int i = 4; i < 16; ++i) { e[i] = __builtin_amdgcn_exp2f(e[i]); PIN(e[i]); }
      SB();
      mx = max3(max3(m0.x, m0.y, m0.z), max3(m0.w, m1.x, m1.y), max3(m1.z, m1.w, m1.w)) * SCL;
      const float fs = __builtin_amdgcn_exp2f(ml - mx);
      f2 ee[8];
#pragma unroll
      for (int i = 0; i < 8; ++i) ee[i] = f2{e[2 * i], e[2 * i + 1]} * fs;
      *LP(v4u, pw) = v4u{pk2(ee[0].x, ee[0].y), pk2(ee[1].x, ee[1].y), pk2(ee[2].x, ee[2].y), pk2(ee[3].x, ee[3].y)};
      *LP(v4u, pw + 1024) = v4u{pk2(ee[4].x, ee[4].y), pk2(ee[5].x, ee[5].y), pk2(ee[6].x, ee[6].y), pk2(ee[7].x, ee[7].y)};
      const f2 s2 = ((ee[0] + ee[1]) + (ee[2] + ee[3])) + ((ee[4] + ee[5]) + (ee[6] + ee[7]));
      *LP(float, mxw + L_LS) = s2.x + s2.y;
      LBAR();
      {
        const f4 s0 = *LP(const f4, mxr + L_LS), s1 = *LP(const f4, mxr + L_LS + 16);
        const float sum = add1(add1(add1(s0.x, s0.y), add1(s0.z, s0.w)), add1(add1(s1.x, s1.y), add1(s1.z, s1.w)));
        sth(gran(fu(mx), fu(sum), e1), rw, mlo);
        if (w == 1 && l == 0) sth2(v2u{e1, tag2(e1)}, rw, W_FLAG + (64 + bk) * 128);
      }
#pragma unroll 1
      for (u32 sp = 0; sp < SPIN_CAP && !__builtin_amdgcn_readfirstlane(*LP(volatile u32, L_HF + w * 4)); ++sp) __builtin_amdgcn_s_sleep(1);
      CBAR();
      v4u rb[4];
#pragma unroll
      for (int n = 0; n < 4; ++n) { const v4u t = *LP(const v4u, sr_ + n * 2048 + (n & 1) * 128); rb[n] = (n & 1) ? v4u{t.z, t.w, t.x, t.y} : t; }
#pragma unroll
      for (int n = 0; n < 4; ++n) sth(guard4(rb[n]), rw, oo, n * 8192);
      RBAR();
    } else {
      const int w4 = w - 4;
      if (w4 < 3) {
#pragma unroll
        for (int i = 0; i < 14; ++i) {
          const int u = w4 * 14 + i;
          sth(sent4, rw, l * 16, u < 32 ? W_OP + bk * 32768 + u * 1024 : W_PO + bk * 10240 + (u - 32) * 1024);
        }
      }
      const int bk2 = (l >> 4) & 1, q = (l & 15) >> 2, pp = l & 3;
      int sw_ = L_SL + w4 * 8192 + (l & 31) * 256 + 16 * (l & 15) + 8 * (hh ^ ((l >> 3) & 1)), pr_ = L_P + l * 16;
      PIN(sw_); PIN(pr_);
#pragma unroll 1
      for (int step = 0; step < 3; ++step) {
        LBAR();
        if (step == 1) QLOAD();
        SB();
        if (step == (w4 < 2 ? 0 : w4 - 1)) {
#pragma unroll
          for (int ct = 0; ct < 4; ++ct)
#pragma unroll
            for (int kk = 0; kk < 8; ++kk) {
              const int r0 = 16 * kk + 4 * hh + q, r1 = r0 + 8, ch = 4 * ct + 2 * bk2 + (pp >> 1);
              LAS unsigned char* base = L + w4 * 32768 + 8 * (pp & 1);
              fr[ct * 8 + kk] = trfrag(base + r0 * 256 + 16 * (ch ^ swz(r0)), base + r1 * 256 + 16 * (ch ^ swz(r1)));
            }
          LWAIT();
#pragma unroll
          for (int s = 0; s < 32; ++s) PIN(fr[s]);
        }
        if (step == 1) { QGATHER(); DRAIN(); }
      }
      u32 cp2 = 0;
      if (w4 < 3) cp2 = code[umin(tid - 256, CODE_LINES - 1) * 32];
      SB();
      RBAR();
      __builtin_amdgcn_s_setprio(3);
      RBAR();
      v4u pf[8];
#pragma unroll
      for (int kk = 0; kk < 8; ++kk) { pf[kk] = *LP(const v4u, pr_ + kk * 1024); SB(); }
      f16v o[4];
#define PV_MM(ct, kk) do { o[ct] = mfma32(fr[(ct) * 8 + (kk)], pf[kk], o[ct]); if (((ct) > 0 && (kk) >= 2 && (kk) <= 5) || ((ct) < 3 && (kk) == 7)) PIN(o[ct]); SB(); } while (0)
#define PV_ZERO(ct) _Pragma("unroll") for (int i = 0; i < 16; ++i) o[ct][i] = 0.f
#define PV_TILE(ct) do { PV_ZERO(ct); PIN(fr[(ct) * 8]); PV_MM(ct, 0); PV_MM(ct, 1); PV_MM(ct, 2); pv_epi<(ct) - 1, 0>(sw_, o[(ct) - 1]); SB(); PV_MM(ct, 3); pv_epi<(ct) - 1, 1>(sw_, o[(ct) - 1]); SB(); \
        PV_MM(ct, 4); pv_epi<(ct) - 1, 2>(sw_, o[(ct) - 1]); SB(); PV_MM(ct, 5); pv_epi<(ct) - 1, 3>(sw_, o[(ct) - 1]); SB(); } while (0)
      PV_ZERO(0);
#pragma unroll
      for (int kk = 0; kk < 8; ++kk) PV_MM(0, kk);
      PV_TILE(1); PV_MM(1, 6); PV_MM(1, 7);
      PV_TILE(2);
      CBAR();
      *LP(volatile u32, L_HF + w4 * 4) = 1u;
      SB();
      PV_MM(2, 6); PV_MM(2, 7);
      PV_TILE(3);
      int sw3[4];
#pragma unroll
      for (int g4 = 0; g4 < 4; ++g4) { sw3[g4] = sw_ ^ ((12 + g4) << 4); PIN(sw3[g4]); }
      PIN(fr[30]);
      PV_MM(3, 6); PV_MM(3, 7);
#pragma unroll
      for (int g4 = 0; g4 < 4; ++g4) *LP(v2u, sw3[g4]) = v2u{pk2(o[3][4 * g4], o[3][4 * g4 + 1]), pk2(o[3][4 * g4 + 2], o[3][4 * g4 + 3])};
      __builtin_amdgcn_s_setprio(0);
      LBAR();
      asm volatile("" :: "v"(cp2));
    }
    {
      const v4u r0 = *LP(const v4u, sr2 + 128), t1 = *LP(const v4u, sr2 + 2048), r1 = {t1.z, t1.w, t1.x, t1.y};
      sth(guard4(r0), rw, oo2);
      sth(guard4(r1), rw, oo2, 8192);
    }
    if (i2 < 40) {
      const int du = i2, db = du / 20;
      float gate;
      {
        const rsrc_t rg = mk(p.gw, M * 2);
        float s = 0.f;
#pragma unroll
        for (int i = 0; i < 5; ++i) {
          const u32 xa = __builtin_amdgcn_raw_buffer_load_b32(rx, (db * M + 128 * i + 2 * l) * 2, 0, 0);
          const u32 ga = __builtin_amdgcn_raw_buffer_load_b32(rg, (128 * i + 2 * l) * 2, 0, 0);
          s += bflo(xa) * bflo(ga) + bfhi(xa) * bfhi(ga);
        }
        s += rorn<1>(s); s += rorn<2>(s); s += rorn<4>(s); s += rorn<8>(s);
        s = swap16add(s, s); s = swap32add(s, s);
        gate = __builtin_amdgcn_rcpf(1.f + __expf(-s));
      }
      PIN(gate);
      const int pr = tid >> 3;
      wait_flags(rw, 64 + 4 * w + ((l >> 3) & 3) + 32 * (l >> 5), e1);
      int lw = 65536 + ((4 * (l & 7) + g) * 8 + w) * 4, lr = 65536 + (l & 31) * 32;
      int o = W_PO + pr * 5120 + du * 128 + (l & 7) * 16, oz = l < 32 ? (32 * du + l) * 2 : 0x40000000;
      const rsrc_t ro = mk(p.out, NB * M * 2);
      f4 v;
      {
        v4u f0, f1;
        const int of = W_PF + (int)umin(pr, 53) * 10240 + du * 256 + (l & 7) * 32;
        POLL((f0 = ldh(rw, of), f1 = ldh(rw, of + 16)), __any(stale(f0, e1) | stale(f1, e1)));
        v = f4{uf(f0.x), uf(f0.z), uf(f1.x), uf(f1.z)} * (1.f - gate);
        if (pr >= 54) v = f4{0.f, 0.f, 0.f, 0.f};
      }
      PIN(v); PIN(lw); PIN(lr); PIN(o); PIN(oz);
      {
        v4u t0, t1;
        POLL((t0 = ldh(rw, o), t1 = ldh(rw, o, 64 * 5120)), __any(umax(max4(t0), max4(t1)) == SENT));
        v += (__builtin_bit_cast(f4, t0) + __builtin_bit_cast(f4, t1)) * gate;
      }
      float s = swap32add(swap16add(v.x, v.y), swap16add(v.z, v.w));
      s += rorn<8>(s);
      *LP(float, lw) = s;
      LBAR();
      if (w == 0) {
        const f4 s0 = *LP(const f4, lr) + *LP(const f4, lr + 16);
        const float t = (s0.x + s0.y) + (s0.z + s0.w);
        __builtin_amdgcn_raw_buffer_store_b16((u16)pk2(t, t), ro, oz, 0, 0);
      }
    } else if (i2 == 63 && w == 0) {
      wait_flags(rw, 64 + l, e1);
      if (l == 0) sth1(e1, rw, W_EPOCH);
    }
    __builtin_amdgcn_endpgm();
    goto done;
  }
  if (bid >= 128 || j < 2) {
    h ^= 1;
    const int cj = j >> 1, dj = j & 1;
    {
      const u16* sv = p.wuv + ((size_t)(256 * cj + 32 * w + (l >> 3)) * H + h) * D + 64 * dj;
      const u16* so = p.wo + ((size_t)h * D + 64 * dj + 8 * w + g) * M;
      const int fo = 2 * (g | ((w & 1) << 2));
#pragma unroll
      for (int n = 0; n < 10; ++n) {
        if (n % 3 == 0) {
          const int m = n / 3, fz = 2 * (((l >> 4) & 1) | ((m & 1) << 1));
          dma(sv + m * 8 * H * D + 8 * ((l & 7) ^ fz), (w * 4 + m) * 1024);
        }
        dma(so + (n & 1) * 4 * M + (n >> 1) * 128 + 8 * (r ^ fo), 32768 + (n >> 1) * 16384 + (8 * w + 4 * (n & 1)) * 256);
      }
    }
    const int cb_b = w >> 2, cb = (w >> 1) & 1, kh = w & 1;
    constexpr int L_A2 = 114688, L_A3 = L_A2 + 4 * 528, L_WT = 118784;
    int a2w = L_A2 + (cb_b * 2 + kh) * 528 + (128 * cb + 8 * r + 4 * (g & 1)) * 2;
    int a2r = L_A2 + (r & 3) * 528 + 16 * g;
    int a3w = L_A3 + (g & 1) * 144 + (16 * (w & 3) + r) * 2;
    int a3r = L_A3 + (r >> 3) * 144 + 16 * g;
    int po = W_PO + (h * 4 + j) * 5120 + ((g >> 1) * M + 32 * w + 16 * (g & 1) + r) * 4;
    int oo = W_OP + (((cb_b * 32 + 16 * kh + g) * H + h) * DC + 256 * cj + 128 * cb + 8 * r) * 2;
    int mo = W_ML + (l * H + h) * 16;
    PIN(a2w); PIN(a2r); PIN(a3w); PIN(a3r); PIN(po); PIN(oo); PIN(mo);
    DRAIN();
    RBAR();
    v4u of[4], ml = {0u, 0u, 0u, 0u};
    if (w == 7) ml = ldh(rw, mo);
    SB();
    v4u wv[8], wo[12];
    const int q = r >> 2, pp = l & 3;
#pragma unroll
    for (int pi = 0; pi < 3; ++pi) {
      const int P = (pi == 2 ? (w & 3) : w) + 8 * pi;
#pragma unroll
      for (int tt = 0; tt < 2; ++tt)
#pragma unroll
        for (int ks = 0; ks < 2; ++ks) {
          const int row = 32 * ks + 8 * g + q, fo = 2 * (q | ((g & 1) << 2));
          LAS unsigned char* a = L + 32768 + (P >> 2) * 16384 + row * 256 + 16 * ((4 * (P & 3) + 2 * tt + (pp >> 1)) ^ fo) + 8 * (pp & 1);
          wo[pi * 4 + tt * 2 + ks] = trfrag(a, a + 1024);
        }
    }
    LWAIT();
#pragma unroll
    for (int i = 0; i < 12; ++i) PIN(wo[i]);
    if (w == 7 && __any(stale(ml, e1))) POLL(ml = ldh(rw, mo), __any(stale(ml, e1)));
    RBAR();
#pragma unroll
    for (int i = 0; i < 4; ++i) of[i] = ldh(rw, oo, i * 4 * H * DC * 2);
    SB();
    if (w == 7) {
      const float mm = uf(ml.x), ll = uf(ml.z);
      float mx = mm;
      mx = fmaxf(mx, rorn<1>(mx)); mx = fmaxf(mx, rorn<2>(mx)); mx = fmaxf(mx, rorn<4>(mx)); mx = fmaxf(mx, rorn<8>(mx));
      { auto t = __builtin_amdgcn_permlane16_swap(fu(mx), fu(mx), false, false); mx = fmaxf(uf(t[0]), uf(t[1])); }
      const float e = __builtin_amdgcn_exp2f(mm - mx);
      float sum = e * ll;
      sum += rorn<1>(sum); sum += rorn<2>(sum); sum += rorn<4>(sum); sum += rorn<8>(sum);
      sum = swap16add(sum, sum);
      const float wgt = e * __builtin_amdgcn_rcpf(sum);
      *LP(u32, L_WT + ((l & 48) + (l & 3) * 4 + ((l >> 2) & 3)) * 4) = pk2(wgt, wgt);
    }
    if (w < 4) {
#pragma unroll
      for (int ks = 0; ks < 8; ++ks) {
        const int row = 32 * ks + 8 * g + q, fz = 2 * (((q >> 1) & 1) | ((g & 1) << 1));
        LAS unsigned char* a = L + row * 128 + 16 * ((2 * (w & 3) + (pp >> 1)) ^ fz) + 8 * (pp & 1);
        wv[ks] = trfrag(a, a + 512);
      }
    } else {
#pragma unroll
      for (int ks = 0; ks < 8; ++ks) asm volatile("" : "=v"(wv[ks]));
    }
    LBAR();
#pragma unroll
    for (int i = 0; i < 8; ++i) PIN(wv[i]);
    v4u wsel[4];
    {
      const v4u wt = *LP(const v4u, L_WT + (cb_b * 32 + kh * 16 + g * 4) * 4);
      wsel[0] = selfrag(wt.x, mask); wsel[1] = selfrag(wt.y, mask); wsel[2] = selfrag(wt.z, mask); wsel[3] = selfrag(wt.w, mask);
#pragma unroll
      for (int i = 0; i < 4; ++i) PIN(wsel[i]);
      SB();
#pragma unroll 1
      for (u32 sp = 0; sp < SPIN_CAP; ++sp) {
        u32 bd = 0;
#pragma unroll
        for (int i = 0; i < 4; ++i) bd |= __any(max4(of[i]) == SENT) ? 1u << i : 0u;
        if (!bd) break;
        CBAR(); SB();
#pragma unroll
        for (int i = 0; i < 4; ++i) if (bd >> i & 1) of[i] = ldh(rw, oo, i * 4 * H * DC * 2);
      }
    }
    {
      f4 acc = {0.f, 0.f, 0.f, 0.f};
#pragma unroll
      for (int i = 0; i < 4; ++i) acc = mfma16(wsel[i], of[i], acc);
      *LP(v2u, a2w) = v2u{pk2(acc.x, acc.y), pk2(acc.z, acc.w)};
    }
    LBAR();
    if (w < 4) {
      v4u af[8];
#pragma unroll
      for (int ks = 0; ks < 8; ++ks) af[ks] = *LP(const v4u, a2r + 64 * ks);
      SB();
      f4 acc = {0.f, 0.f, 0.f, 0.f};
#pragma unroll
      for (int ks = 0; ks < 8; ++ks) acc = mfma16(af[ks], wv[ks], acc);
      const float v = (g & 1) ? acc.z + acc.w : acc.x + acc.y;
      *LP(u16, a3w) = (u16)pk2(v, v);
    }
    LBAR();
    {
      const v4u a0 = *LP(const v4u, a3r), a1 = *LP(const v4u, a3r + 64);
#pragma unroll
      for (int pi = 0; pi < 3; ++pi) {
        if (pi == 2 && w >= 4) break;
        f4 t0 = {0.f, 0.f, 0.f, 0.f}, t1 = {0.f, 0.f, 0.f, 0.f};
        t0 = mfma16(a0, wo[pi * 4 + 0], t0); t1 = mfma16(a0, wo[pi * 4 + 2], t1);
        t0 = mfma16(a1, wo[pi * 4 + 1], t0); t1 = mfma16(a1, wo[pi * 4 + 3], t1);
        sth1(guard(fu((g & 1) ? t1.x : t0.x)), rw, po + pi * 1024);
      }
    }
  } else {
    const int i3 = ((h & 3) == 1 ? 24 + (h >> 2) : (h >> 2) * 3 + ((h & 3) >> 1) + ((h & 3) == 3)) * 2 + j - 2;
    if (i3 < 54) {
      const int pair = i3 >> 1, half = i3 & 1;
      u32 xv[5];
#pragma unroll
      for (int f = 0; f < 5; ++f) xv[f] = __builtin_amdgcn_raw_buffer_load_b32(rx, (bsel * M + 320 * half + 40 * w + 10 * g) * 2 + 4 * f, 0, 0);
      v4u wf[10], wdn[8];
      const GAS u16* src = (const GAS u16*)((r >> 3) ? p.wu : p.wg) + (320 * half + 40 * w + 10 * g) * F + 64 * pair + 8 * (r & 7);
      if (w >= 1 && w < 6) {
        const rsrc_t rd = mk(p.wd, F * M * 2);
        const int vo = ((32 * i3 + 8 * g) * M + 128 * (w - 1) + 8 * r) * 2;
#pragma unroll
        for (int f = 0; f < 8; ++f) wdn[f] = ldw(rd, vo, f * M * 2);
      } else {
#pragma unroll
        for (int f = 0; f < 8; ++f) asm volatile("" : "=v"(wdn[f]));
      }
      SB();
#pragma unroll
      for (int f = 0; f < 10; ++f) { wf[f] = __builtin_nontemporal_load((const GAS v4u*)(src + f * F)); SB(); }
      f4 acc = {0.f, 0.f, 0.f, 0.f};
#pragma unroll
      for (int f = 0; f < 5; ++f) {
        acc = mfma16(selfrag(duplo(xv[f]), mask), wf[2 * f], acc);
        acc = mfma16(selfrag(duphi(xv[f]), mask), wf[2 * f + 1], acc);
      }
      *(LAS f4*)(L + (w * 64 + l) * 16) = acc;
      LBAR();
      if (w == 0) {
        f4 s = *(const LAS f4*)(L + l * 16);
#pragma unroll
        for (int i = 1; i < 8; ++i) s += *(const LAS f4*)(L + (i * 64 + l) * 16);
        sth(gran(fu(s.x), fu(s.y), e1), rw, W_FX + i3 * 2048 + l * 16);
        sth(gran(fu(s.z), fu(s.w), e1), rw, W_FX + i3 * 2048 + 1024 + l * 16);
        v4u z0, z1;
        POLL((z0 = ldh(rw, W_FX + (i3 ^ 1) * 2048 + l * 16), z1 = ldh(rw, W_FX + (i3 ^ 1) * 2048 + 1024 + l * 16)), __any(stale(z0, e1) | stale(z1, e1)));
        s += f4{uf(z0.x), uf(z0.z), uf(z1.x), uf(z1.z)};
        float hv[4];
#pragma unroll
        for (int i = 0; i < 4; ++i) { const float gs = s[i], us = rorn<8>(s[i]); hv[i] = gs * __builtin_amdgcn_rcpf(1.f + __expf(-gs)) * us; }
        if ((r >> 2) == half) *LP(v2u, 8192 + (g >> 1) * 64 + (8 * (r & 3) + 4 * (g & 1)) * 2) = v2u{pk2(hv[0], hv[1]), pk2(hv[2], hv[3])};
      }
      LBAR();
      if (w >= 1 && w < 6) {
        const v4u z = *LP(const v4u, 8192 + bsel * 64 + g * 16);
        const u32 zz[4] = {z.x, z.y, z.z, z.w};
        acc = f4{0.f, 0.f, 0.f, 0.f};
#pragma unroll
        for (int f = 0; f < 4; ++f) {
          acc = mfma16(selfrag(duplo(zz[f]), mask), wdn[2 * f], acc);
          acc = mfma16(selfrag(duphi(zz[f]), mask), wdn[2 * f + 1], acc);
        }
        const int o = W_PF + i3 * 10240 + ((g >> 1) * M + 128 * (w - 1) + 8 * r + 4 * (g & 1)) * 8;
        sth(gran(fu(acc.x), fu(acc.y), e1), rw, o);
        sth(gran(fu(acc.z), fu(acc.w), e1), rw, o + 16);
      }
    }
  }
done:;
}

extern "C" void kernel_launch(void* const* d_in, const int* in_sizes, int n_in,
                              void* d_out, int out_size, void* d_ws, size_t ws_size,
                              hipStream_t stream) {
  Params p;
  p.x = (const u16*)d_in[0]; p.ckv = (const u16*)d_in[1]; p.wq = (const u16*)d_in[2]; p.wuk = (const u16*)d_in[3];
  p.wuv = (const u16*)d_in[4]; p.wo = (const u16*)d_in[5]; p.wg = (const u16*)d_in[7]; p.wu = (const u16*)d_in[8];
  p.wd = (const u16*)d_in[9]; p.gw = (const u16*)d_in[10]; p.out = (u16*)d_out; p.ws = (unsigned char*)d_ws;
  (void)hipFuncSetAttribute((const void*)mk_kernel, hipFuncAttributeMaxDynamicSharedMemorySize, LDS_BYTES);
  mk_kernel<<<256, NT, LDS_BYTES, stream>>>(p);
}
```

```cpp
#include <hip/hip_runtime.h>
#include <stdint.h>

#define STAMPS 0
#define WS_OFF(n) 0
#define TUNE 0
#define KNOB(i) kKnob[i]
#define KSET 0, 0, 5, 0, 0, 2, 0, 0, 0, 0, 0, 0
constexpr int kKnob[12] = {KSET};

typedef unsigned short u16;
typedef unsigned int u32;
typedef unsigned long long u64;
typedef u32 v4u __attribute__((ext_vector_type(4)));
typedef u32 v2u __attribute__((ext_vector_type(2)));
typedef float f4 __attribute__((ext_vector_type(4)));
typedef float f2 __attribute__((ext_vector_type(2)));
typedef float f16v __attribute__((ext_vector_type(16)));
typedef short bf8 __attribute__((ext_vector_type(8)));
typedef short s4 __attribute__((ext_vector_type(4)));
typedef __bf16 bf2 __attribute__((ext_vector_type(2)));
#define GAS __attribute__((address_space(1)))
#define LAS __attribute__((address_space(3)))
#define DI __device__ __forceinline__
typedef __amdgpu_buffer_rsrc_t rsrc_t;

constexpr int NB = 2, H = 32, SK = 4096, D = 128, M = 640, DC = 512, F = 1728;
constexpr int NT = 512;
constexpr int W_EPOCH = 0;
constexpr int W_FLAG = 4096;
constexpr int W_QA = 65536;
constexpr int W_ML = 24576;
constexpr int W_FX = 131072;
constexpr int W_QP = 262144;
constexpr int W_PO = 524288;
constexpr int W_PF = 1179648;
constexpr int W_OP = 2097152;
constexpr int W_END = 4194304;
constexpr int W_DBG = 8388608;
constexpr int LDS_BYTES = 163840;
constexpr int SC1 = 16, NTL = 2;
constexpr u32 SENT = 0xFFFFFFFFu;
constexpr u32 SPIN_CAP = 1u << 20;
#define CODE_PF 16896
constexpr int CODE_PF_BYTES = CODE_PF;
constexpr float SCL = 0.08838834764831845f * 1.4426950408889634f;

struct Params {
  const u16* x; const u16* ckv; const u16* wq; const u16* wuk; const u16* wuv; const u16* wo;
  const u16* wg; const u16* wu; const u16* wd; const u16* gw; u16* out; unsigned char* ws;
};

DI rsrc_t mk(const void* p, int bytes) { return __builtin_amdgcn_make_buffer_rsrc((void*)p, 0, bytes, 0x00020000); }
DI v4u ldw(rsrc_t r, int voff, int soff) { return __builtin_amdgcn_raw_buffer_load_b128(r, voff, soff, NTL); }
DI v4u ldh(rsrc_t r, int voff) { return __builtin_amdgcn_raw_buffer_load_b128(r, voff, 0, SC1); }
DI v4u ldhs(rsrc_t r, int voff, int soff) { return __builtin_amdgcn_raw_buffer_load_b128(r, voff, soff, SC1); }
DI v2u ldh2(rsrc_t r, int voff) { return __builtin_amdgcn_raw_buffer_load_b64(r, voff, 0, SC1); }
DI void sth(v4u v, rsrc_t r, int voff) { __builtin_amdgcn_raw_buffer_store_b128(v, r, voff, 0, SC1); }
DI void sths(v4u v, rsrc_t r, int voff, int soff) { __builtin_amdgcn_raw_buffer_store_b128(v, r, voff, soff, SC1); }
DI void sth2(v2u v, rsrc_t r, int voff) { __builtin_amdgcn_raw_buffer_store_b64(v, r, voff, 0, SC1); }
DI void sth1(u32 v, rsrc_t r, int voff) { __builtin_amdgcn_raw_buffer_store_b32(v, r, voff, 0, SC1); }
DI u32 umin(u32 a, u32 b) { return a < b ? a : b; }
DI u32 umax(u32 a, u32 b) { return a > b ? a : b; }
DI u32 guard(u32 v) { return umin(v, 0xFFFFFFFEu); }
DI v4u guard4(v4u v) { return v4u{guard(v.x), guard(v.y), guard(v.z), guard(v.w)}; }
DI u32 max4(v4u v) { return umax(umax(v.x, v.y), umax(v.z, v.w)); }
DI u32 pk2(float lo, float hi) { bf2 r = {(__bf16)lo, (__bf16)hi}; return __builtin_bit_cast(u32, r); }
DI float bflo(u32 v) { return __uint_as_float(v << 16); }
DI float bfhi(u32 v) { return __uint_as_float(v & 0xFFFF0000u); }
DI f4 mfma16(v4u a, v4u b, f4 c) { return __builtin_amdgcn_mfma_f32_16x16x32_bf16(__builtin_bit_cast(bf8, a), __builtin_bit_cast(bf8, b), c, 0, 0, 0); }
DI f16v mfma32(v4u a, v4u b, f16v c) { return __builtin_amdgcn_mfma_f32_32x32x16_bf16(__builtin_bit_cast(bf8, a), __builtin_bit_cast(bf8, b), c, 0, 0, 0); }
#define CBAR() asm volatile("" ::: "memory")
#define SB() __builtin_amdgcn_sched_barrier(0)
#define PIN(x) asm volatile("" : "+v"(x))
#define DRAIN() asm volatile("s_waitcnt vmcnt(0)" ::: "memory")
#define VMWAIT(n) asm volatile("s_waitcnt vmcnt(" #n ")" ::: "memory")
#define THR(i) do { const int d_ = KNOB(i); if (d_ == 1) VMWAIT(0); else if (d_ == 2) VMWAIT(1); else if (d_ == 3) VMWAIT(2); else if (d_ == 4) VMWAIT(3); else if (d_ == 6) VMWAIT(5); else if (d_ == 8) VMWAIT(7); SB(); } while (0)
#define RBAR() do { CBAR(); __builtin_amdgcn_s_barrier(); CBAR(); } while (0)
#define LBAR() do { asm volatile("s_waitcnt lgkmcnt(0)" ::: "memory"); __builtin_amdgcn_s_barrier(); CBAR(); } while (0)
template <int N> DI float rorn(float v) { return __builtin_bit_cast(float, __builtin_amdgcn_update_dpp(0, __builtin_bit_cast(int, v), 0x120 + N, 0xf, 0xf, false)); }
DI float max3(float a, float b, float c) { float d; asm("v_max3_f32 %0, %1, %2, %3" : "=v"(d) : "v"(a), "v"(b), "v"(c)); return d; }
DI float swap16add(float x, float y) { auto t = __builtin_amdgcn_permlane16_swap(__float_as_uint(x), __float_as_uint(y), false, false); return __uint_as_float(t[0]) + __uint_as_float(t[1]); }
DI float swap32add(float x, float y) { auto t = __builtin_amdgcn_permlane32_swap(__float_as_uint(x), __float_as_uint(y), false, false); return __uint_as_float(t[0]) + __uint_as_float(t[1]); }
DI v4u selfrag(u32 vv, v4u mask) { return v4u{vv & mask.x, vv & mask.y, vv & mask.z, vv & mask.w}; }
DI u32 duplo(u32 v) { return __builtin_amdgcn_perm(v, v, 0x01000100u); }
DI u32 duphi(u32 v) { return __builtin_amdgcn_perm(v, v, 0x03020302u); }
DI int swz(int row) { return ((row & 3) << 2) | ((row >> 2) & 3); }
DI void dma(const u16* src, LAS unsigned char* dst) { __builtin_amdgcn_global_load_lds((const GAS u32*)src, (LAS u32*)dst, 16, 0, NTL); }
DI void dma_asm(const u16* src, u32 lds_addr) {
  u32 keep;
  asm volatile("s_mov_b32 %0, m0\n\ts_mov_b32 m0, %2\n\ts_nop 0\n\tglobal_load_lds_dwordx4 %1, off nt\n\ts_mov_b32 m0, %0" : "=&s"(keep) : "v"(src), "s"(lds_addr) : "memory");
}
DI v4u trfrag(LAS unsigned char* a0, LAS unsigned char* a1) {
  const v2u lo = __builtin_bit_cast(v2u, __builtin_amdgcn_ds_read_tr16_b64_v4i16((LAS s4*)a0));
  const v2u hi = __builtin_bit_cast(v2u, __builtin_amdgcn_ds_read_tr16_b64_v4i16((LAS s4*)a1));
  return v4u{lo.x, lo.y, hi.x, hi.y};
}
DI v2u mktag(u32 e1) { return v2u{e1, e1 * 0x9E3779B1u ^ 0x5bd1e995u}; }
DI u32 epoch1(rsrc_t rw) { return __builtin_amdgcn_readfirstlane(__builtin_amdgcn_raw_buffer_load_b32(rw, W_EPOCH, 0, SC1)) + 1u; }

#define ROLE_OF(b) ((b) < 128 ? ((((b) >> 3) & 2) ? 2 : 0) : (b) < 192 ? 1 : 3)
#define NROLES 4
#define STAMP(k) do { if ((k) == 1 && (0 < 0 || ROLE_OF(bid) == 0 || (0 == 0 && ROLE_OF(bid) == 3 && (k) >= 6) || (0 == 0 && ROLE_OF(bid) == 2 && (k) <= 4))) { const u64 t_ = __builtin_amdgcn_s_memrealtime(); if (l == 0) __hip_atomic_store((GAS u64*)(p.ws + W_DBG) + 256 + bid * 8 + w, t_, __ATOMIC_RELAXED, __HIP_MEMORY_SCOPE_AGENT); } } while (0)

DI void wait_flags(rsrc_t rw, int idx, v2u tag) {
#pragma unroll 1
  for (u32 sp = 0; sp < SPIN_CAP; ++sp) {
    CBAR();
    v2u f = ldh2(rw, W_FLAG + idx * 128);
    if (!__any((f.x != tag.x) | (f.y != tag.y))) break;
    __builtin_amdgcn_s_sleep(1);
  }
  CBAR();
}

__global__ void __launch_bounds__(NT, 2) mk_kernel(Params p) {
  extern __shared__ __attribute__((aligned(16))) unsigned char smem[];
  LAS unsigned char* L = (LAS unsigned char*)smem;
  const int tid = threadIdx.x, l = tid & 63, w = __builtin_amdgcn_readfirstlane(tid >> 6);
  const int bid = blockIdx.x;
  const int r = l & 15, g = l >> 4;
  STAMP(0);
  const u64 t_start = __builtin_amdgcn_s_memrealtime();
#define HOLD(i) do { if (KNOB(i) > 0) while (__builtin_amdgcn_s_memrealtime() - t_start < (u64)KNOB(i)) __builtin_amdgcn_s_sleep(1); } while (0)
  u64 pc0; asm volatile("s_getpc_b64 %0" : "=s"(pc0));
  u32 cpf = 0;
  if (KNOB(9) == 0) { if (w >= 5) cpf = ((const GAS u32*)(pc0 & ~127ull))[umin(tid - 320, CODE_PF_BYTES / 128 - 1) * 32]; }
  else if (KNOB(9) == 1) { if (w >= 3) cpf = __builtin_nontemporal_load((const GAS u32*)(pc0 & ~127ull) + umin(tid - 192, CODE_PF_BYTES / 64 - 1) * 16); }
  else if (KNOB(9) == 2) { if (w >= 3) cpf = ((const GAS u32*)(pc0 & ~127ull))[umin(tid - 192, CODE_PF_BYTES / 64 - 1) * 16]; }
  CBAR(); SB();
  if (__builtin_expect(bid >= 192 && w < 4, 1)) {
    const u16* src = p.wuk + ((size_t)(256 * ((bid >> 3) & 1) + 64 * w + g) * H + (bid & 7) * 4 + ((bid - 192) >> 4)) * D;
#pragma unroll 1
    for (int n = 0; n < 16; ++n) dma_asm(src + n * 4 * H * D + 8 * (r ^ ((2 * n + (g >> 1)) & 15)), (u32)(size_t)L + w * 16384 + n * 1024);
  }
  SB();
  const rsrc_t rw = mk(p.ws, W_END);
  const u32 ep = __builtin_amdgcn_raw_buffer_load_b32(rw, W_EPOCH, 0, SC1);
  SB();
  if (w == 7) { if (l < 8) __hip_atomic_store((GAS u64*)(p.ws + W_DBG) + 256 + bid * 8 + l, t_start, __ATOMIC_RELAXED, __HIP_MEMORY_SCOPE_AGENT); if (l == 8) __hip_atomic_store((GAS u64*)(p.ws + W_DBG) + bid, t_start, __ATOMIC_RELAXED, __HIP_MEMORY_SCOPE_AGENT); }
  const rsrc_t rx = mk(p.x, NB * M * 2);
  const u32 m16 = (l & 1) ? 0xFFFF0000u : 0x0000FFFFu;
  const int jq = (l & 7) >> 1;
  const v4u mask = {jq == 0 ? m16 : 0u, jq == 1 ? m16 : 0u, jq == 2 ? m16 : 0u, jq == 3 ? m16 : 0u};
  const int bsel = (l >> 3) & 1;
  const v4u sent4 = {SENT, SENT, SENT, SENT};
  int h, j;
  u32 e1;
#define CLEAR_BOXES(bx) do { \
    _Pragma("unroll") for (int i = 0; i < 4; ++i) sths(sent4, rw, W_OP + (bx) * 32768 + tid * 16, i * 8192); \
    sths(sent4, rw, W_PO + (bx) * 10240 + tid * 16, 0); \
    if (w < 2) sths(sent4, rw, W_PO + (bx) * 10240 + tid * 16, 8192); \
    DRAIN(); RBAR(); \
    if (tid == 0) sth2(mktag(e1), rw, W_FLAG + (64 + (bx)) * 128); } while (0)

  if (__builtin_expect(bid < 128, 1)) {
    h = (bid & 7) * 4 + (bid >> 5); j = (bid >> 3) & 3;
    const int x = h * 4 + j;
    if (__builtin_expect(w < 2, 1)) {
      const rsrc_t rq = mk(p.wq, M * H * D * 2);
      const int mb = 160 * j + 80 * w + 20 * g;
      u32 xv[10];
#pragma unroll
      for (int f = 0; f < 10; ++f) xv[f] = __builtin_amdgcn_raw_buffer_load_b32(rx, (bsel * M + mb) * 2 + 4 * f, 0, 0);
      v4u wa[20];
      const int vo = (mb * (H * D) + h * D + 8 * r) * 2;
#pragma unroll
      for (int f = 0; f < 20; ++f) { wa[f] = ldw(rq, vo, f * H * D * 2); SB(); }
      STAMP(1);
      SB();
      v4u sel[20];
#pragma unroll
      for (int f = 0; f < 10; ++f) { sel[2 * f] = selfrag(duplo(xv[f]), mask); sel[2 * f + 1] = selfrag(duphi(xv[f]), mask); }
#pragma unroll
      for (int f = 0; f < 20; ++f) PIN(sel[f]);
      e1 = __builtin_amdgcn_readfirstlane(ep) + 1u;
      int o = W_QP + (x * 2 + w) * 1024 + l * 16;
      PIN(o);
      SB();
      f4 acc = {0.f, 0.f, 0.f, 0.f};
#pragma unroll
      for (int f = 0; f < 20; ++f) acc = mfma16(sel[f], wa[f], acc);
      sth(v4u{pk2(acc.x, acc.y), e1, pk2(acc.z, acc.w), e1}, rw, o);
      STAMP(3);
    } else {
      e1 = __builtin_amdgcn_readfirstlane(ep) + 1u;
      asm volatile("" :: "v"(cpf));
    }
    RBAR();
    HOLD(6);
    if (KNOB(2) == 2 && j >= 2) CLEAR_BOXES(h * 2 + j - 2);
    if (KNOB(2) == 4 && j < 2) CLEAR_BOXES(h * 2 + j);
    if (KNOB(5) > 0 && (KNOB(1) == 0 || j < 2)) {
      if (KNOB(1) == 0) {
        const int i2 = (bid & 7) + 8 * (bid >> 4), b = (i2 & 7) >> 2, ks = (i2 & 3) + 4 * (i2 >> 3);
        const u16* src = p.ckv + ((size_t)b * SK + 128 * ks + 64 * ((bid >> 3) & 1) + 8 * w + g) * DC + 8 * r;
#pragma unroll
        for (int blk = 4 - KNOB(5); blk < 4; ++blk) { dma(src + 128 * blk, L + 131072 + w * 2048); dma(src + 128 * blk + 4 * DC, L + 131072 + w * 2048 + 1024); }
      } else {
        const int i2 = (bid & 15) + 16 * (bid >> 5), b = (i2 & 7) >> 2, ks = (i2 & 3) + 4 * (i2 >> 3);
        const u16* src = p.ckv + ((size_t)b * SK + 128 * ks + 16 * w + g) * DC + 8 * r;
#pragma unroll
        for (int blk = 4 - KNOB(5); blk < 4; ++blk)
#pragma unroll
          for (int n = 0; n < 4; ++n) dma(src + 128 * blk + 4 * n * DC, L + 131072 + w * 4096 + n * 1024);
      }
      RBAR();
    }
    if (KNOB(2) == 3 && j >= 2) CLEAR_BOXES(h * 2 + j - 2);
    STAMP(4);
  } else if (bid >= 192) {
    const int y = bid - 192;
    h = (y & 7) * 4 + (y >> 4);
    const int jj = (y >> 3) & 1;
    j = 2 + jj;
    v4u wk[16];
    if (w < 4) {
      DRAIN();
      STAMP(1);
      e1 = __builtin_amdgcn_readfirstlane(ep) + 1u;
    } else {
      __builtin_amdgcn_s_setprio(3);
      if (w == 4) sth(sent4, rw, W_QA + (((l >> 5) * H + h) * DC + 256 * jj) * 2 + (l & 31) * 16);
      DRAIN();
      e1 = __builtin_amdgcn_readfirstlane(ep) + 1u;
      if (w == 4 && l == 0) sth2(mktag(e1), rw, W_FLAG + (h * 2 + jj) * 128);
      __builtin_amdgcn_s_setprio(0);
      asm volatile("" :: "v"(cpf));
      STAMP(1);
    }
    int oa = W_QA + (((g >> 1) * H + h) * DC + 256 * jj + 64 * w + 32 * (g & 1) + 2 * r) * 2;
    int qw = 65536 + ((g >> 1) * 8 + w) * 272 + (8 * r + 4 * (g & 1)) * 2, qr = 65536 + r * 272 + 16 * g;
    int o = W_QP + (h * 8 + w) * 1024 + l * 16;
    PIN(oa); PIN(qw); PIN(qr); PIN(o);
    RBAR();
    STAMP(2);
    {
      v4u u = ldh(rw, o);
      SB();
#pragma unroll
      for (int t = 0; t < 4; ++t)
#pragma unroll
        for (int s = 0; s < 4; ++s) wk[t * 4 + s] = *(const LAS v4u*)(L + (w & 3) * 16384 + (32 * (t >> 1) + 2 * r + (t & 1)) * 256 + 16 * ((4 * s + g) ^ r));
#pragma unroll
      for (int i = 0; i < 16; ++i) PIN(wk[i]);
#pragma unroll 1
      for (u32 sp = 0; sp < SPIN_CAP; ++sp) {
        if (!__any((u.y != e1) | (u.w != e1))) break;
        CBAR();
        u = ldh(rw, o);
      }
      STAMP(3);
      *(LAS v2u*)(L + qw) = v2u{u.x, u.z};
    }
    LBAR();
    STAMP(4);
    if (w < 4) {
      v4u qf[4];
#pragma unroll
      for (int s = 0; s < 4; ++s) qf[s] = *(const LAS v4u*)(L + qr + 64 * s);
      f4 a[4];
#pragma unroll
      for (int t = 0; t < 4; ++t) a[t] = f4{0.f, 0.f, 0.f, 0.f};
#pragma unroll
      for (int s = 0; s < 4; ++s)
#pragma unroll
        for (int t = 0; t < 4; ++t) a[t] = mfma16(qf[s], wk[t * 4 + s], a[t]);
      float st[4];
#pragma unroll
      for (int t = 0; t < 4; ++t) st[t] = (a[t].x + a[t].y) + (a[t].z + a[t].w);
      sth1(guard(pk2(swap16add(st[0], st[2]), swap16add(st[1], st[3]))), rw, oa);
      STAMP(5);
    }
    RBAR();
    if (KNOB(2) == 1) CLEAR_BOXES(h * 2 + jj);
  } else {
    const int i2 = bid - 128;
    const int b = (i2 & 7) >> 2, ks = (i2 & 3) + 4 * (i2 >> 3), bk = b * 32 + ks;
    const int hh = l >> 5;
    constexpr int L_Q = 131072, L_MX = 0, L_LS = 1024, L_P = 2048, L_SL = 10240;
    if (KNOB(2) == 0) { e1 = __builtin_amdgcn_readfirstlane(ep) + 1u; CLEAR_BOXES(bk); }
    v4u fr[32];
    int sr_ = L_SL + (w & 3) * 8704 + (l >> 3) * 272 + 16 * (l & 7);
    int oo = W_OP + bk * 32768 + (l >> 3) * 1024 + (128 * (w & 3) + 8 * (l & 7)) * 2;
    if (__builtin_expect(w < 4, 1)) {
      const u16* src = p.ckv + ((size_t)b * SK + 128 * ks + 32 * w + g) * DC;
      e1 = __builtin_amdgcn_readfirstlane(ep) + 1u;
      asm volatile("" : "+s"(e1));
      HOLD(0);
      v2u fl;
#pragma unroll
      for (int n = 0; n < 32; ++n) {
        const int blk = n >> 3, r0 = 4 * (n & 7);
        dma(src + r0 * DC + 128 * blk + 8 * (r ^ ((g << 2) | (n & 3))), L + blk * 32768 + (32 * w + r0) * 256);
        if (n == 23) {
          SB();
          asm volatile("buffer_load_dwordx2 %0, %1, %2, 0 offen sc1" : "=v"(fl) : "v"(W_FLAG + (16 * w + (l >> 2)) * 128), "s"(rw) : "memory");
          SB();
        }
      }
      STAMP(1);
      const int row = 32 * w + (l & 31), sw = swz(row);
      VMWAIT(17);
      RBAR();
#pragma unroll
      for (int s = 0; s < 16; ++s) fr[s] = *(const LAS v4u*)(L + (s >> 3) * 32768 + row * 256 + 16 * ((2 * (s & 7) + hh) ^ sw));
      SB();
      asm volatile("s_waitcnt vmcnt(8)" : "+v"(fl) :: "memory");
      RBAR();
      {
        const v2u tag = mktag(e1);
        if (__any((fl.x != tag.x) | (fl.y != tag.y))) wait_flags(rw, 16 * w + (l >> 2), tag);
      }
      HOLD(11);
      v4u qv[8];
      const int qo = W_QA + b * 32768 + w * 8192 + l * 16;
#pragma unroll
      for (int i = 0; i < 8; ++i) qv[i] = ldhs(rw, qo, i * 1024);
      SB();
#pragma unroll
      for (int s = 0; s < 8; ++s) fr[16 + s] = *(const LAS v4u*)(L + 2 * 32768 + row * 256 + 16 * ((2 * s + hh) ^ sw));
      SB();
      VMWAIT(8);
      STAMP(2);
#pragma unroll
      for (int s = 0; s < 8; ++s) fr[24 + s] = *(const LAS v4u*)(L + 3 * 32768 + row * 256 + 16 * ((2 * s + hh) ^ sw));
      SB();
#pragma unroll 1
      for (u32 sp = 0; sp < SPIN_CAP; ++sp) {
        u32 mx = 0;
#pragma unroll
        for (int i = 0; i < 8; ++i) {
          mx = umax(mx, max4(qv[i]));
          *(LAS v4u*)(L + L_Q + (8 * w + i) * 1024 + 16 * (l ^ ((8 * w + i) & 15))) = qv[i];
        }
        if (!__any(mx == SENT)) break;
        CBAR();
#pragma unroll
        for (int i = 0; i < 8; ++i) qv[i] = ldhs(rw, qo, i * 1024);
      }
      STAMP(4);
      int qb = L_Q + (l & 31) * 1024;
      const int qx = (l & 15) ^ hh;
      int mxw = L_MX + ((l & 31) * 8 + w * 2 + hh) * 4;
      PIN(qb); PIN(mxw); PIN(sr_); PIN(oo);
#pragma unroll
      for (int s = 0; s < 32; ++s) PIN(fr[s]);
      STAMP(3);
      LBAR();
      STAMP(5);
      f16v sacc;
#pragma unroll
      for (int i = 0; i < 16; ++i) sacc[i] = 0.f;
      v4u qf[12];
#define QADDR(s) (L + qb + ((s) >> 3) * 256 + 16 * (((2 * (s)) & 15) ^ qx))
#pragma unroll
      for (int s = 0; s < 12; ++s) { qf[s] = *(const LAS v4u*)QADDR(s); SB(); }
#pragma unroll
      for (int s = 0; s < 32; ++s) {
        sacc = mfma32(fr[s], qf[s % 12], sacc);
        if (s + 12 < 32) qf[s % 12] = *(const LAS v4u*)QADDR(s + 12);
        SB();
      }
      float mx = fmaxf(fmaxf(sacc[0], sacc[1]), sacc[2]);
      PIN(mx); SB();
      mx = max3(max3(mx, sacc[3], sacc[4]), max3(sacc[5], sacc[6], sacc[7]), max3(max3(sacc[8], sacc[9], sacc[10]), max3(sacc[11], sacc[12], sacc[13]), max3(sacc[14], sacc[15], mx)));
      *(LAS float*)(L + mxw) = mx;
      LBAR();
      STAMP(6);
      const f4 m0 = *(const LAS f4*)(L + L_MX + (l & 31) * 32), m1 = *(const LAS f4*)(L + L_MX + (l & 31) * 32 + 16);
      mx = max3(max3(m0.x, m0.y, m0.z), max3(m0.w, m1.x, m1.y), max3(m1.z, m1.w, m1.w)) * SCL;
      float e[16];
#pragma unroll
      for (int i = 0; i < 16; ++i) e[i] = __builtin_amdgcn_exp2f(__builtin_fmaf(sacc[i], SCL, -mx));
      *(LAS v4u*)(L + L_P + ((2 * w) * 64 + l) * 16) = v4u{pk2(e[0], e[1]), pk2(e[2], e[3]), pk2(e[4], e[5]), pk2(e[6], e[7])};
      *(LAS v4u*)(L + L_P + ((2 * w + 1) * 64 + l) * 16) = v4u{pk2(e[8], e[9]), pk2(e[10], e[11]), pk2(e[12], e[13]), pk2(e[14], e[15])};
      float sum = (((e[0] + e[1]) + (e[2] + e[3])) + ((e[4] + e[5]) + (e[6] + e[7]))) + (((e[8] + e[9]) + (e[10] + e[11])) + ((e[12] + e[13]) + (e[14] + e[15])));
      *(LAS float*)(L + mxw + L_LS) = sum;
      LBAR();
      STAMP(7);
      if (w == 0 && l < 32) {
        const f4 s0 = *(const LAS f4*)(L + L_LS + l * 32), s1 = *(const LAS f4*)(L + L_LS + l * 32 + 16);
        sum = ((s0.x + s0.y) + (s0.z + s0.w)) + ((s1.x + s1.y) + (s1.z + s1.w));
        sth(v4u{__float_as_uint(mx), e1, __float_as_uint(sum), e1}, rw, W_ML + bk * 512 + l * 16);
      }
      if (KNOB(2) == 5 && w == 1 && l == 0) sth2(mktag(e1), rw, W_FLAG + (64 + bk) * 128);
      RBAR();
      v4u rb[4];
#pragma unroll
      for (int n = 0; n < 4; ++n) rb[n] = *(const LAS v4u*)(L + sr_ + n * 8 * 272);
#pragma unroll
      for (int n = 0; n < 4; ++n) sths(rb[n], rw, oo, n * 8192);
      RBAR();
    } else {
      const int w4 = w - 4;
      e1 = __builtin_amdgcn_readfirstlane(ep) + 1u;
      asm volatile("" :: "v"(cpf));
      const int bk2 = (l >> 4) & 1, q = (l & 15) >> 2, pp = l & 3;
#define VFRAGS() do { \
      _Pragma("unroll") for (int ct = 0; ct < 4; ++ct) _Pragma("unroll") for (int kk = 0; kk < 8; ++kk) { \
          const int r0 = 16 * kk + 4 * hh + q, r1 = r0 + 8, ch = 4 * ct + 2 * bk2 + (pp >> 1); \
          LAS unsigned char* base = L + w4 * 32768 + 8 * (pp & 1); \
          fr[ct * 8 + kk] = trfrag(base + r0 * 256 + 16 * (ch ^ swz(r0)), base + r1 * 256 + 16 * (ch ^ swz(r1))); } \
      _Pragma("unroll") for (int s = 0; s < 32; ++s) PIN(fr[s]); } while (0)
      const v2u tag = mktag(e1);
      v2u fl = tag;
      const int nb = w4 < 2 ? 1 : w4;
#pragma unroll 1
      for (int k = 0; k < nb; ++k) RBAR();
      if (w4 == 3) { STAMP(5); if (KNOB(2) != 5) fl = ldh2(rw, W_FLAG + (64 + bk) * 128); }
      SB();
      VFRAGS();
      if (w4 == 3 && __any((fl.x != tag.x) | (fl.y != tag.y))) wait_flags(rw, 64 + bk, tag);
      STAMP(3);
      asm volatile("s_waitcnt lgkmcnt(0)" ::: "memory");
#pragma unroll 1
      for (int k = nb; k < 3; ++k) RBAR();
      int sw_ = L_SL + w4 * 8704 + (l & 31) * 272 + 8 * hh;
      PIN(sw_); PIN(sr_); PIN(oo);
      if (KNOB(2) == 5) {
        if (w4 < 3) {
#pragma unroll
          for (int i = 0; i < 14; ++i) {
            const int u = w4 * 14 + i;
            sths(sent4, rw, l * 16, u < 32 ? W_OP + bk * 32768 + u * 1024 : W_PO + bk * 10240 + (u - 32) * 1024);
          }
        }
        STAMP(1);
      }
      RBAR();
      if (KNOB(2) == 5) { DRAIN(); STAMP(2); }
      RBAR();
      STAMP(7);
      v4u pf[8];
#pragma unroll
      for (int kk = 0; kk < 8; ++kk) pf[kk] = *(const LAS v4u*)(L + L_P + (kk * 64 + l) * 16);
      SB();
      f16v o[4];
#pragma unroll
      for (int ct = 0; ct < 5; ++ct) {
        if (ct < 4) {
#pragma unroll
          for (int i = 0; i < 16; ++i) o[ct][i] = 0.f;
        }
#pragma unroll
        for (int kk = 0; kk < 8; ++kk) {
          if (ct < 4) o[ct] = mfma32(fr[ct * 8 + kk], pf[kk], o[ct]);
          SB();
          if (ct > 0 && kk >= 1 && kk < 5) {
            const int g4 = kk - 1, pt = ct - 1;
            *(LAS v2u*)(L + sw_ + (32 * pt + 8 * g4) * 2) = v2u{guard(pk2(o[pt][4 * g4], o[pt][4 * g4 + 1])), guard(pk2(o[pt][4 * g4 + 2], o[pt][4 * g4 + 3]))};
          }
          if (ct == 2 && kk == 6) LBAR();
          SB();
        }
      }
      STAMP(6);
      LBAR();
    }
    {
      const int half = (w >> 2) * 2;
      const v4u r0 = *(const LAS v4u*)(L + sr_ + half * 8 * 272 + 128), r1 = *(const LAS v4u*)(L + sr_ + (half + 1) * 8 * 272 + 128);
      sth(r0, rw, oo + half * 8192 + 128);
      sth(r1, rw, oo + half * 8192 + 8192 + 128);
    }
    STAMP(8);
    if (i2 < 40) {
      const int du = i2, db = du / 20;
      float gate;
      {
        const rsrc_t rg = mk(p.gw, M * 2);
        float s = 0.f;
#pragma unroll
        for (int i = 0; i < 5; ++i) {
          const u32 xa = __builtin_amdgcn_raw_buffer_load_b32(rx, (db * M + 128 * i + 2 * l) * 2, 0, 0);
          const u32 ga = __builtin_amdgcn_raw_buffer_load_b32(rg, (128 * i + 2 * l) * 2, 0, 0);
          s += bflo(xa) * bflo(ga) + bfhi(xa) * bfhi(ga);
        }
        s += rorn<1>(s); s += rorn<2>(s); s += rorn<4>(s); s += rorn<8>(s);
        s = swap16add(s, s); s = swap32add(s, s);
        gate = 1.f / (1.f + __expf(-s));
      }
      const int pr = tid >> 3;
      wait_flags(rw, 64 + 4 * w + ((l >> 3) & 3) + 32 * (l >> 5), mktag(e1));
      int lw = ((4 * (l & 7) + g) * 8 + w) * 4;
      PIN(lw);
      LAS unsigned char* LD = L + 65536;
      STAMP(9);
      f4 v;
      {
        v4u t0, t1, f0, f1;
        const int o = W_PO + pr * 5120 + du * 128 + (l & 7) * 16;
        const int of = W_PF + (int)umin(pr, 53) * 10240 + du * 256 + (l & 7) * 32;
        bool fok = false;
#pragma unroll 1
        for (u32 sp = 0; sp < SPIN_CAP; ++sp) {
          CBAR();
          t0 = ldh(rw, o); t1 = ldhs(rw, o, 64 * 5120);
          if (!fok) {
            f0 = ldh(rw, of); f1 = ldh(rw, of + 16);
            fok = !__any((f0.y != e1) | (f0.w != e1) | (f1.y != e1) | (f1.w != e1));
          }
          if (fok && !__any(umax(max4(t0), max4(t1)) == SENT)) break;
        }
        const float gf = pr < 54 ? 1.f - gate : 0.f;
        v = f4{__uint_as_float(f0.x), __uint_as_float(f0.z), __uint_as_float(f1.x), __uint_as_float(f1.z)} * gf + (__builtin_bit_cast(f4, t0) + __builtin_bit_cast(f4, t1)) * gate;
      }
      STAMP(10);
      float s = swap32add(swap16add(v.x, v.y), swap16add(v.z, v.w));
      s += rorn<8>(s);
      *(LAS float*)(LD + lw) = s;
      LBAR();
      if (w == 0) {
        const f4 s0 = *(const LAS f4*)(LD + (l & 31) * 32), s1 = *(const LAS f4*)(LD + (l & 31) * 32 + 16);
        const float t = ((s0.x + s0.y) + (s0.z + s0.w)) + ((s1.x + s1.y) + (s1.z + s1.w));
        if (l < 32) p.out[32 * du + l] = (u16)pk2(t, t);
        if (du == 0 && l == 0) sth1(e1, rw, W_EPOCH);
        if (du == 0) {
          const u64 now = __builtin_amdgcn_s_memrealtime();
          u64 mn = ~0ull, mx = 0;
          for (int i = 0; i < 4; ++i) { const u64 t = __hip_atomic_load((GAS u64*)(p.ws + W_DBG) + i * 64 + l, __ATOMIC_RELAXED, __HIP_MEMORY_SCOPE_AGENT); mn = t < mn ? t : mn; }
          for (int i = 0; i < 32; ++i) { const u64 t = __hip_atomic_load((GAS u64*)(p.ws + W_DBG) + 256 + i * 64 + l, __ATOMIC_RELAXED, __HIP_MEMORY_SCOPE_AGENT); mx = t > mx ? t : mx; }
          for (int o = 1; o < 64; o <<= 1) { const u64 a = __shfl_xor(mn, o), b2 = __shfl_xor(mx, o); mn = a < mn ? a : mn; mx = b2 > mx ? b2 : mx; }
          u64 extra = 300 + (1 == 99 ? 0 : 1 == 98 ? 2 * (now - mn) : 2 * (mx - mn));
          if (extra > 10000) extra = 10000;
          while (__builtin_amdgcn_s_memrealtime() < now + extra) {}
        }
      }
      STAMP(11);
    }
    goto done;
  }
  if (bid >= 128 || j < 2) {
    const int ac = h * 4 + j;
    HOLD(3);
    const int cj = j >> 1, dj = j & 1;
    {
      const u16* sv = p.wuv + ((size_t)(256 * cj + 32 * w + (l >> 3)) * H + h) * D + 64 * dj;
#pragma unroll
      for (int n = 0; n < 4; ++n) {
        const int fz = 2 * (((l >> 4) & 1) | ((n & 1) << 1));
        if (n < KNOB(10) && bid < 128) THR(7);
        dma(sv + n * 8 * H * D + 8 * ((l & 7) ^ fz), L + (w * 4 + n) * 1024);
      }
      const u16* so = p.wo + ((size_t)h * D + 64 * dj + 8 * w + g) * M;
      const int fo = 2 * (g | ((w & 1) << 2));
#pragma unroll
      for (int n = 0; n < 10; ++n) { if (n + 4 < KNOB(10) && bid < 128) THR(7); dma(so + (n & 1) * 4 * M + (n >> 1) * 128 + 8 * (r ^ fo), L + 32768 + (n >> 1) * 16384 + (8 * w + 4 * (n & 1)) * 256); }
    }
    const int cb_b = w >> 2, cb = (w >> 1) & 1, kh = w & 1;
    constexpr int L_A2 = 114688, L_A3 = L_A2 + 4 * 528, L_WT = 118784;
    int a2w = L_A2 + (cb_b * 2 + kh) * 528 + (128 * cb + 8 * r + 4 * (g & 1)) * 2;
    int a2r = L_A2 + (r & 3) * 528 + 16 * g;
    int a3w = L_A3 + (g & 1) * 144 + (16 * (w & 3) + r) * 2;
    int a3r = L_A3 + (r >> 3) * 144 + 16 * g;
    int po = W_PO + ac * 5120 + ((g >> 1) * M + 32 * w + 16 * (g & 1) + r) * 4;
    int oo = W_OP + (((cb_b * 32 + 16 * kh + g) * H + h) * DC + 256 * cj + 128 * cb + 8 * r) * 2;
    PIN(a2w); PIN(a2r); PIN(a3w); PIN(a3r); PIN(po); PIN(oo);
    if (KNOB(2) != 5) wait_flags(rw, 64 + 8 * w + (l >> 3), mktag(e1));
    DRAIN();
    RBAR();
    STAMP(6);
    v4u of[4], ml = {0u, 0u, 0u, 0u};
    if (w == 7) ml = ldh(rw, W_ML + (l * H + h) * 16);
#pragma unroll
    for (int i = 0; i < 4; ++i) of[i] = KNOB(2) == 5 ? sent4 : ldhs(rw, oo, i * 4 * H * DC * 2);
    SB();
    v4u wv[8], wo[12];
    {
      const int q = r >> 2, pp = l & 3;
#pragma unroll
      for (int ks = 0; ks < 8; ++ks) {
        const int row = 32 * ks + 8 * g + q, fz = 2 * (((q >> 1) & 1) | ((g & 1) << 1));
        LAS unsigned char* a = L + row * 128 + 16 * ((2 * (w & 3) + (pp >> 1)) ^ fz) + 8 * (pp & 1);
        wv[ks] = trfrag(a, a + 512);
      }
#pragma unroll
      for (int pi = 0; pi < 3; ++pi) {
        const int P = (pi == 2 ? (w & 3) : w) + 8 * pi;
#pragma unroll
        for (int tt = 0; tt < 2; ++tt)
#pragma unroll
          for (int ks = 0; ks < 2; ++ks) {
            const int row = 32 * ks + 8 * g + q, fo = 2 * (q | ((g & 1) << 2));
            LAS unsigned char* a = L + 32768 + (P >> 2) * 16384 + row * 256 + 16 * ((4 * (P & 3) + 2 * tt + (pp >> 1)) ^ fo) + 8 * (pp & 1);
            wo[pi * 4 + tt * 2 + ks] = trfrag(a, a + 1024);
          }
      }
#pragma unroll
      for (int i = 0; i < 8; ++i) PIN(wv[i]);
#pragma unroll
      for (int i = 0; i < 12; ++i) PIN(wo[i]);
    }
    STAMP(11);
    if (w == 7) {
#pragma unroll 1
      for (u32 sp = 0; sp < SPIN_CAP; ++sp) {
        if (!__any((ml.y != e1) | (ml.w != e1))) break;
        CBAR();
        ml = ldh(rw, W_ML + (l * H + h) * 16);
      }
      STAMP(10);
      const float mm = __uint_as_float(ml.x), ll = __uint_as_float(ml.z);
      float mx = mm;
      mx = fmaxf(mx, rorn<1>(mx)); mx = fmaxf(mx, rorn<2>(mx)); mx = fmaxf(mx, rorn<4>(mx)); mx = fmaxf(mx, rorn<8>(mx));
      { auto t = __builtin_amdgcn_permlane16_swap(__float_as_uint(mx), __float_as_uint(mx), false, false); mx = fmaxf(__uint_as_float(t[0]), __uint_as_float(t[1])); }
      const float e = __builtin_amdgcn_exp2f(mm - mx);
      float sum = e * ll;
      sum += rorn<1>(sum); sum += rorn<2>(sum); sum += rorn<4>(sum); sum += rorn<8>(sum);
      sum = swap16add(sum, sum);
      const float wgt = e * __builtin_amdgcn_rcpf(sum);
      *(LAS u32*)(L + L_WT + ((l & 48) + (l & 3) * 4 + ((l >> 2) & 3)) * 4) = pk2(wgt, wgt);
    }
    LBAR();
    STAMP(7);
    v4u wsel[4];
    {
      const v4u wt = *(const LAS v4u*)(L + L_WT + (cb_b * 32 + kh * 16 + g * 4) * 4);
      bool bad = __any(umax(umax(max4(of[0]), max4(of[1])), umax(max4(of[2]), max4(of[3]))) == SENT);
      if (bad) {
#pragma unroll
        for (int i = 0; i < 4; ++i) of[i] = ldhs(rw, oo, i * 4 * H * DC * 2);
      }
      SB();
      wsel[0] = selfrag(wt.x, mask); wsel[1] = selfrag(wt.y, mask); wsel[2] = selfrag(wt.z, mask); wsel[3] = selfrag(wt.w, mask);
#pragma unroll
      for (int i = 0; i < 4; ++i) PIN(wsel[i]);
      SB();
#pragma unroll 1
      for (u32 sp = 0; sp < SPIN_CAP; ++sp) {
        u32 bd = 0;
#pragma unroll
        for (int i = 0; i < 4; ++i) bd |= __any(max4(of[i]) == SENT) ? 1u << i : 0u;
        if (!bd) break;
        CBAR(); SB();
#pragma unroll
        for (int i = 0; i < 4; ++i) if (bd >> i & 1) of[i] = ldhs(rw, oo, i * 4 * H * DC * 2);
      }
    }
    STAMP(8);
    {
      f4 acc = {0.f, 0.f, 0.f, 0.f};
#pragma unroll
      for (int i = 0; i < 4; ++i) acc = mfma16(wsel[i], of[i], acc);
      *(LAS v2u*)(L + a2w) = v2u{pk2(acc.x, acc.y), pk2(acc.z, acc.w)};
    }
    LBAR();
    if (w < 4) {
      v4u af[8];
#pragma unroll
      for (int ks = 0; ks < 8; ++ks) af[ks] = *(const LAS v4u*)(L + a2r + 64 * ks);
      SB();
      f4 acc = {0.f, 0.f, 0.f, 0.f};
#pragma unroll
      for (int ks = 0; ks < 8; ++ks) acc = mfma16(af[ks], wv[ks], acc);
      const float v = (g & 1) ? acc.z + acc.w : acc.x + acc.y;
      *(LAS u16*)(L + a3w) = (u16)pk2(v, v);
    }
    LBAR();
    {
      const v4u a0 = *(const LAS v4u*)(L + a3r), a1 = *(const LAS v4u*)(L + a3r + 64);
#pragma unroll
      for (int pi = 0; pi < 3; ++pi) {
        if (pi == 2 && w >= 4) break;
        f4 t0 = {0.f, 0.f, 0.f, 0.f}, t1 = {0.f, 0.f, 0.f, 0.f};
        t0 = mfma16(a0, wo[pi * 4 + 0], t0); t1 = mfma16(a0, wo[pi * 4 + 2], t1);
        t0 = mfma16(a1, wo[pi * 4 + 1], t0); t1 = mfma16(a1, wo[pi * 4 + 3], t1);
        sth1(guard(__float_as_uint((g & 1) ? t1.x : t0.x)), rw, po + pi * 1024);
      }
    }
    STAMP(9);
  } else {
    const int i3 = h * 2 + j - 2;
    HOLD(4);
    if (i3 < 54) {
      const int pair = i3 >> 1, half = i3 & 1;
      STAMP(6);
      u32 xv[5];
#pragma unroll
      for (int f = 0; f < 5; ++f) xv[f] = __builtin_amdgcn_raw_buffer_load_b32(rx, (bsel * M + 320 * half + 40 * w + 10 * g) * 2 + 4 * f, 0, 0);
      v4u wf[10], wdn[8];
      const GAS u16* src = (const GAS u16*)((r >> 3) ? p.wu : p.wg) + (320 * half + 40 * w + 10 * g) * F + 64 * pair + 8 * (r & 7);
      if (w >= 1 && w < 6) {
        const rsrc_t rd = mk(p.wd, F * M * 2);
        const int vo = ((32 * i3 + 8 * g) * M + 128 * (w - 1) + 8 * r) * 2;
#pragma unroll
        for (int f = 0; f < 8; ++f) { if (f < KNOB(10)) THR(8); wdn[f] = ldw(rd, vo, f * M * 2); }
      } else {
#pragma unroll
        for (int f = 0; f < 8; ++f) wdn[f] = v4u{0u, 0u, 0u, 0u};
      }
      SB();
#pragma unroll
      for (int f = 0; f < 10; ++f) { if (f < KNOB(10) && (w == 0 || w >= 6)) THR(8); wf[f] = __builtin_nontemporal_load((const GAS v4u*)(src + f * F)); SB(); }
      f4 acc = {0.f, 0.f, 0.f, 0.f};
#pragma unroll
      for (int f = 0; f < 5; ++f) {
        acc = mfma16(selfrag(duplo(xv[f]), mask), wf[2 * f], acc);
        acc = mfma16(selfrag(duphi(xv[f]), mask), wf[2 * f + 1], acc);
      }
      *(LAS f4*)(L + (w * 64 + l) * 16) = acc;
      STAMP(7);
      LBAR();
      if (w == 0) {
        f4 s = *(const LAS f4*)(L + l * 16);
#pragma unroll
        for (int i = 1; i < 8; ++i) s += *(const LAS f4*)(L + (i * 64 + l) * 16);
        sth(v4u{__float_as_uint(s.x), e1, __float_as_uint(s.y), e1}, rw, W_FX + i3 * 2048 + l * 16);
        sth(v4u{__float_as_uint(s.z), e1, __float_as_uint(s.w), e1}, rw, W_FX + i3 * 2048 + 1024 + l * 16);
        v4u z0, z1;
#pragma unroll 1
        for (u32 sp = 0; sp < SPIN_CAP; ++sp) {
          CBAR();
          z0 = ldh(rw, W_FX + (i3 ^ 1) * 2048 + l * 16); z1 = ldh(rw, W_FX + (i3 ^ 1) * 2048 + 1024 + l * 16);
          if (!__any((z0.y != e1) | (z0.w != e1) | (z1.y != e1) | (z1.w != e1))) break;
        }
        const f4 o = {__uint_as_float(z0.x), __uint_as_float(z0.z), __uint_as_float(z1.x), __uint_as_float(z1.z)};
        s += o;
        float hv[4];
#pragma unroll
        for (int i = 0; i < 4; ++i) { const float gs = s[i], us = rorn<8>(s[i]); hv[i] = gs * __builtin_amdgcn_rcpf(1.f + __expf(-gs)) * us; }
        if ((r >> 2) == half) *(LAS v2u*)(L + 8192 + (g >> 1) * 64 + (8 * (r & 3) + 4 * (g & 1)) * 2) = v2u{pk2(hv[0], hv[1]), pk2(hv[2], hv[3])};
      }
      LBAR();
      if (w >= 1 && w < 6) {
        const v4u z = *(const LAS v4u*)(L + 8192 + bsel * 64 + g * 16);
        const u32 zz[4] = {z.x, z.y, z.z, z.w};
        acc = f4{0.f, 0.f, 0.f, 0.f};
#pragma unroll
        for (int f = 0; f < 4; ++f) {
          acc = mfma16(selfrag(duplo(zz[f]), mask), wdn[2 * f], acc);
          acc = mfma16(selfrag(duphi(zz[f]), mask), wdn[2 * f + 1], acc);
        }
        const int o = W_PF + i3 * 10240 + ((g >> 1) * M + 128 * (w - 1) + 8 * r + 4 * (g & 1)) * 8;
        sth(v4u{__float_as_uint(acc.x), e1, __float_as_uint(acc.y), e1}, rw, o);
        sth(v4u{__float_as_uint(acc.z), e1, __float_as_uint(acc.w), e1}, rw, o + 16);
      }
      STAMP(8);
    }
  }
done:;
}

extern "C" void kernel_launch(void* const* d_in, const int* in_sizes, int n_in,
                              void* d_out, int out_size, void* d_ws, size_t ws_size,
                              hipStream_t stream) {
  Params p;
  p.x = (const u16*)d_in[0]; p.ckv = (const u16*)d_in[1]; p.wq = (const u16*)d_in[2]; p.wuk = (const u16*)d_in[3];
  p.wuv = (const u16*)d_in[4]; p.wo = (const u16*)d_in[5]; p.wg = (const u16*)d_in[7]; p.wu = (const u16*)d_in[8];
  p.wd = (const u16*)d_in[9]; p.gw = (const u16*)d_in[10]; p.out = (u16*)d_out; p.ws = (unsigned char*)d_ws + WS_OFF(ws_size);
  (void)hipFuncSetAttribute((const void*)mk_kernel, hipFuncAttributeMaxDynamicSharedMemorySize, LDS_BYTES);
  mk_kernel<<<256, NT, LDS_BYTES, stream>>>(p);
}
```

```cpp
#include <hip/hip_runtime.h>
#include <stdint.h>


typedef unsigned short u16;
typedef unsigned int u32;
typedef unsigned long long u64;
typedef u32 v4u __attribute__((ext_vector_type(4)));
typedef u32 v2u __attribute__((ext_vector_type(2)));
typedef float f4 __attribute__((ext_vector_type(4)));
typedef float f2 __attribute__((ext_vector_type(2)));
typedef float f16v __attribute__((ext_vector_type(16)));
typedef short bf8 __attribute__((ext_vector_type(8)));
typedef short s4 __attribute__((ext_vector_type(4)));
typedef __bf16 bf2 __attribute__((ext_vector_type(2)));
#define GAS __attribute__((address_space(1)))
#define LAS __attribute__((address_space(3)))
#define DI __device__ __forceinline__
typedef __amdgpu_buffer_rsrc_t rsrc_t;

constexpr int NB = 2, H = 32, SK = 4096, D = 128, M = 640, DC = 512, F = 1728;
constexpr int NT = 512;
constexpr int W_EPOCH = 0;
constexpr int W_FLAG = 4096;
constexpr int W_ML = 24576;
constexpr int W_QA = 65536;
constexpr int W_FX = 131072;
constexpr int W_QP = 262144;
constexpr int W_PO = 524288;
constexpr int W_PF = 1179648;
constexpr int W_OP = 2097152;
constexpr int W_END = 4194304;
constexpr int LDS_BYTES = 163840;
constexpr int SC1 = 16, NTL = 2;
constexpr u32 SENT = 0xFFFFFFFFu;
constexpr u32 SPIN_CAP = 1u << 20;
constexpr int CODE_LINES = 18048 / 128;
constexpr int TILE_HOLD = 80;
constexpr int HELP_BLOCKS = 2;
constexpr float SCL = 0.08838834764831845f * 1.4426950408889634f;

struct Params {
  const u16* x; const u16* ckv; const u16* wq; const u16* wuk; const u16* wuv; const u16* wo;
  const u16* wg; const u16* wu; const u16* wd; const u16* gw; u16* out; unsigned char* ws;
};

DI rsrc_t mk(const void* p, int bytes) { return __builtin_amdgcn_make_buffer_rsrc((void*)p, 0, bytes, 0x00020000); }
DI v4u ldw(rsrc_t r, int voff, int soff) { return __builtin_amdgcn_raw_buffer_load_b128(r, voff, soff, NTL); }
DI v4u ldh(rsrc_t r, int voff, int soff = 0) { return __builtin_amdgcn_raw_buffer_load_b128(r, voff, soff, SC1); }
DI v2u ldh2(rsrc_t r, int voff) { return __builtin_amdgcn_raw_buffer_load_b64(r, voff, 0, SC1); }
DI void sth(v4u v, rsrc_t r, int voff, int soff = 0) { __builtin_amdgcn_raw_buffer_store_b128(v, r, voff, soff, SC1); }
DI void sth2(v2u v, rsrc_t r, int voff) { __builtin_amdgcn_raw_buffer_store_b64(v, r, voff, 0, SC1); }
DI void sth1(u32 v, rsrc_t r, int voff) { __builtin_amdgcn_raw_buffer_store_b32(v, r, voff, 0, SC1); }
DI u32 umin(u32 a, u32 b) { return a < b ? a : b; }
DI u32 umax(u32 a, u32 b) { return a > b ? a : b; }
DI u32 guard(u32 v) { return umin(v, 0xFFFFFFFEu); }
DI v4u guard4(v4u v) { return v4u{guard(v.x), guard(v.y), guard(v.z), guard(v.w)}; }
DI u32 max4(v4u v) { return umax(umax(v.x, v.y), umax(v.z, v.w)); }
DI u32 fu(float v) { return __float_as_uint(v); }
DI float uf(u32 v) { return __uint_as_float(v); }
DI u32 pk2(float lo, float hi) { bf2 r = {(__bf16)lo, (__bf16)hi}; return __builtin_bit_cast(u32, r); }
DI float bflo(u32 v) { return uf(v << 16); }
DI float bfhi(u32 v) { return uf(v & 0xFFFF0000u); }
DI f4 mfma16(v4u a, v4u b, f4 c) { return __builtin_amdgcn_mfma_f32_16x16x32_bf16(__builtin_bit_cast(bf8, a), __builtin_bit_cast(bf8, b), c, 0, 0, 0); }
DI f16v mfma32(v4u a, v4u b, f16v c) { return __builtin_amdgcn_mfma_f32_32x32x16_bf16(__builtin_bit_cast(bf8, a), __builtin_bit_cast(bf8, b), c, 0, 0, 0); }
#define CBAR() asm volatile("" ::: "memory")
#define SB() __builtin_amdgcn_sched_barrier(0)
#define PIN(x) asm volatile("" : "+v"(x))
#define DRAIN() asm volatile("s_waitcnt vmcnt(0)" ::: "memory")
#define VMWAIT(n) asm volatile("s_waitcnt vmcnt(" #n ")" ::: "memory")
#define LWAIT() asm volatile("s_waitcnt lgkmcnt(0)" ::: "memory")
#define RBAR() do { CBAR(); __builtin_amdgcn_s_barrier(); CBAR(); } while (0)
#define LBAR() do { LWAIT(); __builtin_amdgcn_s_barrier(); CBAR(); } while (0)
#define LP(T, off) ((LAS T*)(size_t)(off))
#define POLL(LOADS, BAD) do { u32 sp_ = SPIN_CAP; _Pragma("unroll 1") do { CBAR(); LOADS; } while ((BAD) && --sp_); CBAR(); } while (0)
template <int N> DI float rorn(float v) { return __builtin_bit_cast(float, __builtin_amdgcn_update_dpp(0, __builtin_bit_cast(int, v), 0x120 + N, 0xf, 0xf, false)); }
DI float max3(float a, float b, float c) { float d; asm("v_max3_f32 %0, %1, %2, %3" : "=v"(d) : "v"(a), "v"(b), "v"(c)); return d; }
DI float add1(float a, float b) { float d; asm("v_add_f32 %0, %1, %2" : "=v"(d) : "v"(a), "v"(b)); return d; }
DI float swap16add(float x, float y) { auto t = __builtin_amdgcn_permlane16_swap(fu(x), fu(y), false, false); return uf(t[0]) + uf(t[1]); }
DI float swap32add(float x, float y) { auto t = __builtin_amdgcn_permlane32_swap(fu(x), fu(y), false, false); return uf(t[0]) + uf(t[1]); }
DI v4u selfrag(u32 vv, v4u mask) { return v4u{vv & mask.x, vv & mask.y, vv & mask.z, vv & mask.w}; }
DI u32 duplo(u32 v) { return __builtin_amdgcn_perm(v, v, 0x01000100u); }
DI u32 duphi(u32 v) { return __builtin_amdgcn_perm(v, v, 0x03020302u); }
DI int swz(int row) { return ((row & 3) << 2) | ((row >> 2) & 3); }
template <int AUX> DI void dma(const u16* src, int lds) { __builtin_amdgcn_global_load_lds((const GAS u32*)src, LP(u32, lds), 16, 0, AUX); }
template <int AUX> DI void dma_asm(const u16* src, u32 lds) {
  u32 keep;
  if (AUX) asm volatile("s_mov_b32 %0, m0\n\ts_mov_b32 m0, %2\n\ts_nop 0\n\tglobal_load_lds_dwordx4 %1, off nt\n\ts_mov_b32 m0, %0" : "=&s"(keep) : "v"(src), "s"(lds) : "memory");
  else asm volatile("s_mov_b32 %0, m0\n\ts_mov_b32 m0, %2\n\ts_nop 0\n\tglobal_load_lds_dwordx4 %1, off\n\ts_mov_b32 m0, %0" : "=&s"(keep) : "v"(src), "s"(lds) : "memory");
}
DI v4u trfrag(LAS unsigned char* a0, LAS unsigned char* a1) {
  const v2u lo = __builtin_bit_cast(v2u, __builtin_amdgcn_ds_read_tr16_b64_v4i16((LAS s4*)a0));
  const v2u hi = __builtin_bit_cast(v2u, __builtin_amdgcn_ds_read_tr16_b64_v4i16((LAS s4*)a1));
  return v4u{lo.x, lo.y, hi.x, hi.y};
}
template <int CT, int KK> DI void pv_epi(int addr, const f16v& o) {
  u32 t0, t1;
  asm volatile("v_cvt_pk_bf16_f32 %0, %2, %3\n\tv_cvt_pk_bf16_f32 %1, %4, %5"
               : "=&v"(t0), "=&v"(t1) : "v"(o[4 * KK]), "v"(o[4 * KK + 1]), "v"(o[4 * KK + 2]), "v"(o[4 * KK + 3]), "v"(o));
  *LP(v2u, addr ^ ((4 * CT + KK) << 4)) = v2u{t0, t1};
}
DI u32 tag2(u32 e1) { return e1 * 0x9E3779B1u + 0x5bd1e995u; }
DI v4u gran(u32 a, u32 b, u32 e1) { return v4u{a, e1, b, tag2(e1)}; }
DI bool stale(v4u v, u32 e1) { return (v.y != e1) | (v.w != tag2(e1)); }
DI bool stale2(v2u v, u32 e1) { return (v.x != e1) | (v.y != tag2(e1)); }


DI void wait_flags(rsrc_t rw, int idx, u32 e1) {
#pragma unroll 1
  for (u32 sp = 0; sp < SPIN_CAP; ++sp) {
    CBAR();
    if (!__any(stale2(ldh2(rw, W_FLAG + idx * 128), e1))) break;
    __builtin_amdgcn_s_sleep(1);
  }
  CBAR();
}

typedef u32 v16u __attribute__((ext_vector_type(16)));
typedef u32 v8u __attribute__((ext_vector_type(8)));
template <typename T> DI T* mkptr(u32 lo, u32 hi) { return (T*)(((u64)hi << 32) | lo); }

__global__ void __launch_bounds__(NT, 2) mk_kernel(Params) {
  v16u ka; v8u kb;
  asm volatile("s_load_dwordx16 %0, %2, 0x0\n\ts_load_dwordx8 %1, %2, 0x40" : "=&s"(ka), "=&s"(kb) : "s"(__builtin_amdgcn_kernarg_segment_ptr()));
  extern __shared__ __attribute__((aligned(16))) unsigned char smem[];
  LAS unsigned char* L = (LAS unsigned char*)smem;
  const int tid = threadIdx.x, l = tid & 63, w = __builtin_amdgcn_readfirstlane(tid >> 6);
  const int bid = blockIdx.x;
  const int r = l & 15, g = l >> 4;
  u64 pc0; asm volatile("s_getpc_b64 %0" : "=s"(pc0));
  u32 cpf = 0;
  if (w >= 5) cpf = ((const GAS u32*)(pc0 & ~127ull))[umin(tid - 320, CODE_LINES - 1) * 32];
  const bool isx = bid < 128;
  int h = (bid & 7) * 4 + ((isx ? bid >> 5 : bid >> 4) & 3), j = (bid >> 3) & 3;
  int vo = ((isx ? 160 * j + 80 * w : 256 * (j & 1) + 64 * w) + (isx ? 20 : 16) * g) * (H * D * 2) + h * D * 2 + (isx ? 16 * r : 0);
  const u32 m16 = (l & 1) ? 0xFFFF0000u : 0x0000FFFFu;
  const int jq = (l & 7) >> 1;
  v4u mask = {jq == 0 ? m16 : 0u, jq == 1 ? m16 : 0u, jq == 2 ? m16 : 0u, jq == 3 ? m16 : 0u};
  PIN(vo); PIN(mask);
  asm volatile("s_waitcnt lgkmcnt(0)" : "+s"(ka), "+s"(kb) :: "memory");
  Params p;
  p.x = mkptr<const u16>(ka[0], ka[1]); p.ckv = mkptr<const u16>(ka[2], ka[3]); p.wq = mkptr<const u16>(ka[4], ka[5]); p.wuk = mkptr<const u16>(ka[6], ka[7]);
  p.wuv = mkptr<const u16>(ka[8], ka[9]); p.wo = mkptr<const u16>(ka[10], ka[11]); p.wg = mkptr<const u16>(ka[12], ka[13]); p.wu = mkptr<const u16>(ka[14], ka[15]);
  p.wd = mkptr<const u16>(kb[0], kb[1]); p.gw = mkptr<const u16>(kb[2], kb[3]); p.out = mkptr<u16>(kb[4], kb[5]); p.ws = mkptr<unsigned char>(kb[6], kb[7]);
  SB();
  if (__builtin_expect(bid >= 192 && w < 4, 1)) {
    const u16* src = (const u16*)((const unsigned char*)p.wuk + vo);
#pragma unroll 1
    for (int n = 0; n < 16; ++n) dma_asm<NTL>(src + n * H * D + 8 * (r ^ ((8 * g + (n >> 1)) & 15)), w * 16384 + n * 1024);
  }
  SB();
  const rsrc_t rw = mk(p.ws, W_END), rx = mk(p.x, NB * M * 2);
  const int bsel = (l >> 3) & 1;
  const v4u sent4 = {SENT, SENT, SENT, SENT};
  u32 e1;

  if (__builtin_expect(isx, 1)) {
    if (__builtin_expect(w < 2, 1)) {
      const rsrc_t rq = mk(p.wq, M * H * D * 2);
      v4u wa[20];
      u32 xv[10];
#pragma unroll
      for (int f = 0; f < 20; ++f) {
        wa[f] = ldw(rq, vo, f * H * D * 2);
        SB();
        if (f == 10) {
#pragma unroll
          for (int i = 0; i < 10; ++i) xv[i] = __builtin_amdgcn_raw_buffer_load_b32(rx, (bsel * M + 160 * j + 80 * w + 20 * g) * 2 + 4 * i, 0, 0);
          SB();
        }
      }
      v4u sel[20];
#pragma unroll
      for (int f = 0; f < 10; ++f) { sel[2 * f] = selfrag(duplo(xv[f]), mask); sel[2 * f + 1] = selfrag(duphi(xv[f]), mask); }
#pragma unroll
      for (int f = 0; f < 20; ++f) PIN(sel[f]);
      int o = W_QP + ((h * 4 + j) * 2 + w) * 1024 + l * 16;
      PIN(o);
      SB();
      f4 acc = {0.f, 0.f, 0.f, 0.f};
#pragma unroll
      for (int f = 0; f < 12; ++f) acc = mfma16(sel[f], wa[f], acc);
      PIN(acc);
      RBAR();
      e1 = __builtin_amdgcn_readfirstlane(*LP(const u32, 131064));
      asm volatile("" : "+s"(e1));
      SB();
#pragma unroll
      for (int f = 12; f < 20; ++f) acc = mfma16(sel[f], wa[f], acc);
      sth(gran(pk2(acc.x, acc.y), pk2(acc.z, acc.w), e1), rw, o);
    } else {
      if (w == 2) {
        *LP(u32, 131064) = __builtin_amdgcn_raw_buffer_load_b32(rw, W_EPOCH, 0, SC1) + 1u;
      }
      LBAR();
      asm volatile("" :: "v"(cpf));
    }
    LBAR();
    e1 = __builtin_amdgcn_readfirstlane(*LP(const u32, 131064));
    if (j < 2) {
      const int i2 = (bid & 15) + 16 * (bid >> 5), b = (i2 & 7) >> 2, ks = (i2 & 3) + 4 * (i2 >> 3);
      const u16* src = p.ckv + ((size_t)b * SK + 128 * ks + 16 * w + g) * DC + 8 * r;
#pragma unroll
      for (int blk = 4 - HELP_BLOCKS; blk < 4; ++blk) {
#pragma unroll
        for (int n = 0; n < 4; ++n) dma_asm<0>(src + 128 * blk + 4 * n * DC, 131072 + w * 4096 + n * 1024);
        RBAR();
      }
    }
  } else if (bid >= 192) {
    const int jj = j & 1;
    j = 2 + jj;
    const u32 ep = __builtin_amdgcn_raw_buffer_load_b32(rw, W_EPOCH, 0, SC1);
    if (w >= 4) __builtin_amdgcn_s_setprio(3);
    if (w == 4) sth(sent4, rw, W_QA + (((l >> 5) * H + h) * DC + 256 * jj) * 2 + (l & 31) * 16);
    DRAIN();
    e1 = __builtin_amdgcn_readfirstlane(ep) + 1u;
    if (w == 4 && l == 0) sth2(v2u{e1, tag2(e1)}, rw, W_FLAG + (h * 2 + jj) * 128);
    __builtin_amdgcn_s_setprio(0);
    asm volatile("" :: "v"(cpf));
    int oa = W_QA + (((g >> 1) * H + h) * DC + 256 * jj + 64 * w + 32 * (g & 1) + 2 * r) * 2;
    int qw = 65536 + ((g >> 1) * 8 + w) * 272 + (8 * r + 4 * (g & 1)) * 2, qr = 65536 + r * 272 + 16 * g;
    int o = W_QP + (h * 8 + w) * 1024 + l * 16;
    PIN(oa); PIN(qw); PIN(qr); PIN(o);
    RBAR();
    v4u u = ldh(rw, o), wk[16];
    SB();
#pragma unroll
    for (int t = 0; t < 4; ++t)
#pragma unroll
      for (int s = 0; s < 4; ++s) wk[t * 4 + s] = *(const LAS v4u*)(L + (w & 3) * 16384 + ((2 * r + (t & 1)) & 15) * 1024 + (2 * (t >> 1) + (r >> 3)) * 256 + 16 * ((4 * s + g) ^ r));
    LWAIT();
#pragma unroll
    for (int i = 0; i < 16; ++i) PIN(wk[i]);
    if (__any(stale(u, e1))) POLL(u = ldh(rw, o), __any(stale(u, e1)));
    *LP(v2u, qw) = v2u{u.x, u.z};
    LBAR();
    if (w < 4) {
      v4u qf[4];
#pragma unroll
      for (int s = 0; s < 4; ++s) qf[s] = *LP(const v4u, qr + 64 * s);
      f4 a[4];
#pragma unroll
      for (int t = 0; t < 4; ++t) a[t] = f4{0.f, 0.f, 0.f, 0.f};
#pragma unroll
      for (int s = 0; s < 4; ++s)
#pragma unroll
        for (int t = 0; t < 4; ++t) a[t] = mfma16(qf[s], wk[t * 4 + s], a[t]);
      float st[4];
#pragma unroll
      for (int t = 0; t < 4; ++t) st[t] = (a[t].x + a[t].y) + (a[t].z + a[t].w);
      sth1(guard(pk2(swap16add(st[0], st[2]), swap16add(st[1], st[3]))), rw, oa);
    }
    RBAR();
  } else {
    const int i2 = bid - 128;
    const int b = (i2 & 7) >> 2, ks = (i2 & 3) + 4 * (i2 >> 3), bk = b * 32 + ks;
    const int hh = l >> 5;
    constexpr int L_Q = 131072, L_MX = 0, L_LS = 1024, L_P = 2048, L_HF = 10240, L_SL = 16384;
    const u64 t_start = __builtin_amdgcn_s_memrealtime();
    const u32 ep = __builtin_amdgcn_raw_buffer_load_b32(rw, W_EPOCH, 0, SC1);
    SB();
#pragma unroll
    for (int i = 0; i < 4; ++i) sth(sent4, rw, W_OP + bk * 32768 + tid * 16, i * 8192);
    sth(sent4, rw, W_PO + bk * 10240 + tid * 16);
    if (w < 2) sth(sent4, rw, W_PO + bk * 10240 + tid * 16, 8192);
    else if (w == 2 && l < 40) sth(sent4, rw, l < 32 ? W_ML + bk * 512 + l * 16 : W_FLAG + (64 + bk) * 128 + (l - 32) * 16);
    SB();
    e1 = __builtin_amdgcn_readfirstlane(ep) + 1u;
    asm volatile("" : "+s"(e1) : "v"(cpf), "s"(t_start));
    v4u fr[32], qv[4];
    int sr_ = L_SL + (w & 3) * 8192 + (l >> 3) * 256 + 16 * ((l & 7) ^ (l >> 3));
    int oo = W_OP + bk * 32768 + (l >> 3) * 1024 + (128 * (w & 3) + 8 * (l & 7)) * 2;
    int sr2 = sr_ + (w >> 2) * 4096, oo2 = oo + (w >> 2) * 16384 + 128;
    int qo = W_QA + (b * 32 + w * 4) * 1024 + l * 16, qw[4];
#pragma unroll
    for (int i = 0; i < 4; ++i) { qw[i] = L_Q + (4 * w + i) * 1024 + 16 * (l ^ ((4 * w + i) & 15)); PIN(qw[i]); }
    PIN(sr2); PIN(oo2); PIN(qo);
#define QLOAD() _Pragma("unroll") for (int i = 0; i < 4; ++i) qv[i] = ldh(rw, qo, i * 1024)
#define QBAD() __any(umax(umax(max4(qv[0]), max4(qv[1])), umax(max4(qv[2]), max4(qv[3]))) == SENT)
#define QGATHER() do { if (QBAD()) POLL(QLOAD(), QBAD()); _Pragma("unroll") for (int i = 0; i < 4; ++i) *LP(v4u, qw[i]) = qv[i]; } while (0)
    if (__builtin_expect(w < 4, 1)) {
      const u16* src = p.ckv + ((size_t)b * SK + 128 * ks + 32 * w + g) * DC;
      while (__builtin_amdgcn_s_memrealtime() - t_start < (u64)TILE_HOLD) __builtin_amdgcn_s_sleep(1);
#define TILE_DMA(lo, hi) _Pragma("unroll 1") for (int n = lo; n < hi; ++n) \
        dma_asm<NTL>(src + 4 * (n & 7) * DC + 128 * (n >> 3) + 8 * (r ^ ((g << 2) | (n & 3))), (n >> 3) * 32768 + (32 * w + 4 * (n & 7)) * 256)
      TILE_DMA(0, 16);
      v2u fl;
      asm volatile("buffer_load_dwordx2 %0, %1, %2, 0 offen sc1" : "=v"(fl) : "v"(W_FLAG + (16 * w + (l >> 2)) * 128), "s"(rw) : "memory");
      TILE_DMA(16, 32);
      const int kb = (32 * w + (l & 31)) * 256, sw = swz(l & 31);
      int qa[8];
#pragma unroll
      for (int i = 0; i < 8; ++i) { qa[i] = L_Q + (l & 31) * 1024 + 16 * ((2 * i) ^ (l & 15) ^ hh); PIN(qa[i]); }
      int mxw = L_MX + ((l & 31) * 8 + w * 2 + hh) * 4, mxr = L_MX + (l & 31) * 32, pw = L_P + (2 * w * 64 + l) * 16;
      int mlo = tid < 32 ? W_ML + bk * 512 + l * 16 : 0x40000000;
      PIN(mxw); PIN(mxr); PIN(pw); PIN(sr_); PIN(oo); PIN(mlo);
#define KFRAGS(lo, hi) _Pragma("unroll") for (int s = lo; s < hi; ++s) fr[s] = *(const LAS v4u*)(L + (s >> 3) * 32768 + kb + 16 * ((2 * (s & 7) + hh) ^ sw))
      asm volatile("s_waitcnt vmcnt(16)" : "+v"(fl) :: "memory");
      if (__any(stale2(fl, e1))) wait_flags(rw, 16 * w + (l >> 2), e1);
      RBAR();
      QLOAD();
      SB();
      KFRAGS(0, 16);
      SB();
      VMWAIT(12);
      KFRAGS(16, 24);
      SB();
      VMWAIT(4);
      KFRAGS(24, 32);
      LWAIT();
#pragma unroll
      for (int s = 0; s < 32; ++s) PIN(fr[s]);
      SB();
      QGATHER();
      DRAIN();
      LBAR();
      f16v sacc;
#pragma unroll
      for (int i = 0; i < 16; ++i) sacc[i] = 0.f;
      v4u qf[12];
#pragma unroll
      for (int s = 0; s < 12; ++s) { qf[s] = *LP(const v4u, qa[s & 7] + (s >> 3) * 256); SB(); }
#pragma unroll
      for (int s = 0; s < 32; ++s) {
        sacc = mfma32(fr[s], qf[s % 12], sacc);
        if (s + 12 < 32) qf[s % 12] = *LP(const v4u, qa[(s + 12) & 7] + ((s + 12) >> 3) * 256);
        SB();
      }
      float mx = fmaxf(fmaxf(sacc[0], sacc[1]), sacc[2]);
      PIN(mx); PIN(sacc); SB();
      mx = max3(max3(mx, sacc[3], sacc[4]), max3(sacc[5], sacc[6], sacc[7]), max3(max3(sacc[8], sacc[9], sacc[10]), max3(sacc[11], sacc[12], sacc[13]), max3(sacc[14], sacc[15], mx)));
      *LP(float, mxw) = mx;
      *LP(volatile u32, L_HF + w * 4) = 0u;
      SB();
      float ml = mx * SCL;
      PIN(ml);
      float e[16];
#pragma unroll
      for (int i = 0; i < 16; ++i) e[i] = __builtin_fmaf(sacc[i], SCL, -ml);
#pragma unroll
      for (int i = 0; i < 4; ++i) e[i] = __builtin_amdgcn_exp2f(e[i]);
#pragma unroll
      for (int i = 0; i < 16; ++i) PIN(e[i]);
      SB();
      LBAR();
      const f4 m0 = *LP(const f4, mxr), m1 = *LP(const f4, mxr + 16);
      SB();
#pragma unroll
      for (int i = 4; i < 16; ++i) { e[i] = __builtin_amdgcn_exp2f(e[i]); PIN(e[i]); }
      SB();
      mx = max3(max3(m0.x, m0.y, m0.z), max3(m0.w, m1.x, m1.y), max3(m1.z, m1.w, m1.w)) * SCL;
      const float fs = __builtin_amdgcn_exp2f(ml - mx);
      f2 ee[8];
#pragma unroll
      for (int i = 0; i < 8; ++i) ee[i] = f2{e[2 * i], e[2 * i + 1]} * fs;
      *LP(v4u, pw) = v4u{pk2(ee[0].x, ee[0].y), pk2(ee[1].x, ee[1].y), pk2(ee[2].x, ee[2].y), pk2(ee[3].x, ee[3].y)};
      *LP(v4u, pw + 1024) = v4u{pk2(ee[4].x, ee[4].y), pk2(ee[5].x, ee[5].y), pk2(ee[6].x, ee[6].y), pk2(ee[7].x, ee[7].y)};
      const f2 s2 = ((ee[0] + ee[1]) + (ee[2] + ee[3])) + ((ee[4] + ee[5]) + (ee[6] + ee[7]));
      *LP(float, mxw + L_LS) = s2.x + s2.y;
      LBAR();
      {
        const f4 s0 = *LP(const f4, mxr + L_LS), s1 = *LP(const f4, mxr + L_LS + 16);
        const float sum = add1(add1(add1(s0.x, s0.y), add1(s0.z, s0.w)), add1(add1(s1.x, s1.y), add1(s1.z, s1.w)));
        sth(gran(fu(mx), fu(sum), e1), rw, mlo);
        if (w == 1 && l == 0) sth2(v2u{e1, tag2(e1)}, rw, W_FLAG + (64 + bk) * 128);
      }
#pragma unroll 1
      for (u32 sp = 0; sp < SPIN_CAP && !__builtin_amdgcn_readfirstlane(*LP(volatile u32, L_HF + w * 4)); ++sp) __builtin_amdgcn_s_sleep(1);
      CBAR();
      v4u rb[4];
#pragma unroll
      for (int n = 0; n < 4; ++n) { const v4u t = *LP(const v4u, sr_ + n * 2048 + (n & 1) * 128); rb[n] = (n & 1) ? v4u{t.z, t.w, t.x, t.y} : t; }
#pragma unroll
      for (int n = 0; n < 4; ++n) sth(guard4(rb[n]), rw, oo, n * 8192);
      RBAR();
    } else {
      const int w4 = w - 4;
      const int bk2 = (l >> 4) & 1, q = (l & 15) >> 2, pp = l & 3;
      int sw_ = L_SL + w4 * 8192 + (l & 31) * 256 + 16 * (l & 15) + 8 * (hh ^ ((l >> 3) & 1)), pr_ = L_P + l * 16;
      PIN(sw_); PIN(pr_);
#pragma unroll 1
      for (int step = 0; step < 2; ++step) {
        LBAR();
        if (step == 0) QLOAD();
        SB();
        if (step == (w4 >> 1)) {
#pragma unroll
          for (int ct = 0; ct < 4; ++ct)
#pragma unroll
            for (int kk = 0; kk < 8; ++kk) {
              const int r0 = 16 * kk + 4 * hh + q, r1 = r0 + 8, ch = 4 * ct + 2 * bk2 + (pp >> 1);
              LAS unsigned char* base = L + w4 * 32768 + 8 * (pp & 1);
              fr[ct * 8 + kk] = trfrag(base + r0 * 256 + 16 * (ch ^ swz(r0)), base + r1 * 256 + 16 * (ch ^ swz(r1)));
            }
          LWAIT();
#pragma unroll
          for (int s = 0; s < 32; ++s) PIN(fr[s]);
        }
        if (step == 0) { QGATHER(); DRAIN(); }
      }
      RBAR();
      __builtin_amdgcn_s_setprio(3);
      RBAR();
      v4u pf[8];
#pragma unroll
      for (int kk = 0; kk < 8; ++kk) { pf[kk] = *LP(const v4u, pr_ + kk * 1024); SB(); }
      f16v o[4];
#define PV_MM(ct, kk) do { o[ct] = mfma32(fr[(ct) * 8 + (kk)], pf[kk], o[ct]); if (((ct) > 0 && (kk) >= 2 && (kk) <= 5) || ((ct) < 3 && (kk) == 7)) PIN(o[ct]); SB(); } while (0)
#define PV_ZERO(ct) _Pragma("unroll") for (int i = 0; i < 16; ++i) o[ct][i] = 0.f
#define PV_TILE(ct) do { PV_ZERO(ct); PIN(fr[(ct) * 8]); PV_MM(ct, 0); PV_MM(ct, 1); PV_MM(ct, 2); pv_epi<(ct) - 1, 0>(sw_, o[(ct) - 1]); SB(); PV_MM(ct, 3); pv_epi<(ct) - 1, 1>(sw_, o[(ct) - 1]); SB(); \
        PV_MM(ct, 4); pv_epi<(ct) - 1, 2>(sw_, o[(ct) - 1]); SB(); PV_MM(ct, 5); pv_epi<(ct) - 1, 3>(sw_, o[(ct) - 1]); SB(); } while (0)
      PV_ZERO(0);
#pragma unroll
      for (int kk = 0; kk < 8; ++kk) PV_MM(0, kk);
      PV_TILE(1); PV_MM(1, 6); PV_MM(1, 7);
      PV_TILE(2);
      CBAR();
      *LP(volatile u32, L_HF + w4 * 4) = 1u;
      SB();
      PV_MM(2, 6); PV_MM(2, 7);
      PV_TILE(3);
      int sw3[4];
#pragma unroll
      for (int g4 = 0; g4 < 4; ++g4) { sw3[g4] = sw_ ^ ((12 + g4) << 4); PIN(sw3[g4]); }
      PIN(fr[30]);
      PV_MM(3, 6); PV_MM(3, 7);
#pragma unroll
      for (int g4 = 0; g4 < 4; ++g4) *LP(v2u, sw3[g4]) = v2u{pk2(o[3][4 * g4], o[3][4 * g4 + 1]), pk2(o[3][4 * g4 + 2], o[3][4 * g4 + 3])};
      __builtin_amdgcn_s_setprio(0);
      LBAR();
    }
    {
      const v4u r0 = *LP(const v4u, sr2 + 128), t1 = *LP(const v4u, sr2 + 2048), r1 = {t1.z, t1.w, t1.x, t1.y};
      sth(guard4(r0), rw, oo2);
      sth(guard4(r1), rw, oo2, 8192);
    }
    if (i2 < 40) {
      const int du = i2, db = du / 20;
      RBAR();
      float gate;
      {
        const rsrc_t rg = mk(p.gw, M * 2);
        float s = 0.f;
#pragma unroll
        for (int i = 0; i < 5; ++i) {
          const u32 xa = __builtin_amdgcn_raw_buffer_load_b32(rx, (db * M + 128 * i + 2 * l) * 2, 0, 0);
          const u32 ga = __builtin_amdgcn_raw_buffer_load_b32(rg, (128 * i + 2 * l) * 2, 0, 0);
          s += bflo(xa) * bflo(ga) + bfhi(xa) * bfhi(ga);
        }
        s += rorn<1>(s); s += rorn<2>(s); s += rorn<4>(s); s += rorn<8>(s);
        s = swap16add(s, s); s = swap32add(s, s);
        gate = __builtin_amdgcn_rcpf(1.f + __expf(-s));
      }
      PIN(gate);
      const int pr = tid >> 3;
      wait_flags(rw, 64 + 4 * w + ((l >> 3) & 3) + 32 * (l >> 5), e1);
      int lw = 65536 + ((4 * (l & 7) + g) * 8 + w) * 4, lr = 65536 + (l & 31) * 32;
      int o = W_PO + pr * 5120 + du * 128 + (l & 7) * 16, oz = l < 32 ? (32 * du + l) * 2 : 0x40000000;
      const rsrc_t ro = mk(p.out, NB * M * 2);
      PIN(lw); PIN(lr); PIN(o); PIN(oz);
      f4 v;
      v4u t0, t1;
      {
        v4u f0, f1;
        const int of = W_PF + (int)umin(pr, 53) * 10240 + ((du >> 2) * 2 + (l & 1)) * 256 + (4 * (du & 3) + ((l & 7) >> 1)) * 16;
        POLL((f0 = ldh(rw, of), f1 = ldh(rw, of, 5120), t0 = ldh(rw, o), t1 = ldh(rw, o, 64 * 5120)), __any(stale(f0, e1) | stale(f1, e1)));
        v = f4{uf(f0.x), uf(f0.z), uf(f1.x), uf(f1.z)} * (1.f - gate);
        if (pr >= 54) v = f4{0.f, 0.f, 0.f, 0.f};
      }
      PIN(v);
      if (__any(umax(max4(t0), max4(t1)) == SENT)) POLL((t0 = ldh(rw, o), t1 = ldh(rw, o, 64 * 5120)), __any(umax(max4(t0), max4(t1)) == SENT));
      v += (__builtin_bit_cast(f4, t0) + __builtin_bit_cast(f4, t1)) * gate;
      float s = swap32add(swap16add(v.x, v.y), swap16add(v.z, v.w));
      s += rorn<8>(s);
      *LP(float, lw) = s;
      LBAR();
      if (w == 0) {
        const f4 s0 = *LP(const f4, lr) + *LP(const f4, lr + 16);
        const float t = (s0.x + s0.y) + (s0.z + s0.w);
        __builtin_amdgcn_raw_buffer_store_b16((u16)pk2(t, t), ro, oz, 0, 0);
      }
    } else if (i2 == 63 && w == 0) {
      wait_flags(rw, 64 + l, e1);
      if (l == 0) sth1(e1, rw, W_EPOCH);
    }
    __builtin_amdgcn_endpgm();
    goto done;
  }
  if (bid >= 128 || j < 2) {
    h ^= 1;
    const int cj = j >> 1, dj = j & 1;
    {
      const u16* sv = p.wuv + ((size_t)(256 * cj + 32 * w + (l >> 3)) * H + h) * D + 64 * dj;
      const u16* so = p.wo + ((size_t)h * D + 64 * dj + 8 * w + g) * M;
      const int fo = 2 * (g | ((w & 1) << 2));
#pragma unroll
      for (int n = 0; n < 10; ++n) {
        if (n % 3 == 0) {
          const int m = n / 3, fz = 2 * (((l >> 4) & 1) | ((m & 1) << 1));
          dma<NTL>(sv + m * 8 * H * D + 8 * ((l & 7) ^ fz), (w * 4 + m) * 1024);
        }
        dma<0>(so + (n & 1) * 4 * M + (n >> 1) * 128 + 8 * (r ^ fo), 32768 + (n >> 1) * 16384 + (8 * w + 4 * (n & 1)) * 256);
      }
    }
    const int cb_b = w >> 2, cb = (w >> 1) & 1, kh = w & 1;
    constexpr int L_A2 = 114688, L_A3 = L_A2 + 4 * 528, L_WT = 118784;
    int a2w = L_A2 + (cb_b * 2 + kh) * 528 + (128 * cb + 8 * r + 4 * (g & 1)) * 2;
    int a2r = L_A2 + (r & 3) * 528 + 16 * g;
    int a3w = L_A3 + (g & 1) * 144 + (16 * (w & 3) + r) * 2;
    int a3r = L_A3 + (r >> 3) * 144 + 16 * g;
    int po = W_PO + (h * 4 + j) * 5120 + ((g >> 1) * M + 32 * w + 16 * (g & 1) + r) * 4;
    int oo = W_OP + (((cb_b * 32 + 16 * kh + g) * H + h) * DC + 256 * cj + 128 * cb + 8 * r) * 2;
    int mo = W_ML + (l * H + h) * 16;
    PIN(a2w); PIN(a2r); PIN(a3w); PIN(a3r); PIN(po); PIN(oo); PIN(mo);
    DRAIN();
    RBAR();
    v4u of[4], ml = {0u, 0u, 0u, 0u};
    if (w == 7) ml = ldh(rw, mo);
    SB();
    v4u wv[8], wo[12];
    const int q = r >> 2, pp = l & 3;
#pragma unroll
    for (int pi = 0; pi < 3; ++pi) {
      const int P = (pi == 2 ? (w & 3) : w) + 8 * pi;
#pragma unroll
      for (int tt = 0; tt < 2; ++tt)
#pragma unroll
        for (int ks = 0; ks < 2; ++ks) {
          const int row = 32 * ks + 8 * g + q, fo = 2 * (q | ((g & 1) << 2));
          LAS unsigned char* a = L + 32768 + (P >> 2) * 16384 + row * 256 + 16 * ((4 * (P & 3) + 2 * tt + (pp >> 1)) ^ fo) + 8 * (pp & 1);
          wo[pi * 4 + tt * 2 + ks] = trfrag(a, a + 1024);
        }
    }
    LWAIT();
#pragma unroll
    for (int i = 0; i < 12; ++i) PIN(wo[i]);
    if (w == 7 && __any(stale(ml, e1))) POLL(ml = ldh(rw, mo), __any(stale(ml, e1)));
    RBAR();
#pragma unroll
    for (int i = 0; i < 4; ++i) of[i] = ldh(rw, oo, i * 4 * H * DC * 2);
    SB();
    if (w == 7) {
      const float mm = uf(ml.x), ll = uf(ml.z);
      float mx = mm;
      mx = fmaxf(mx, rorn<1>(mx)); mx = fmaxf(mx, rorn<2>(mx)); mx = fmaxf(mx, rorn<4>(mx)); mx = fmaxf(mx, rorn<8>(mx));
      { auto t = __builtin_amdgcn_permlane16_swap(fu(mx), fu(mx), false, false); mx = fmaxf(uf(t[0]), uf(t[1])); }
      const float e = __builtin_amdgcn_exp2f(mm - mx);
      float sum = e * ll;
      sum += rorn<1>(sum); sum += rorn<2>(sum); sum += rorn<4>(sum); sum += rorn<8>(sum);
      sum = swap16add(sum, sum);
      const float wgt = e * __builtin_amdgcn_rcpf(sum);
      *LP(u32, L_WT + ((l & 48) + (l & 3) * 4 + ((l >> 2) & 3)) * 4) = pk2(wgt, wgt);
    }
    if (w < 4) {
#pragma unroll
      for (int ks = 0; ks < 8; ++ks) {
        const int row = 32 * ks + 8 * g + q, fz = 2 * (((q >> 1) & 1) | ((g & 1) << 1));
        LAS unsigned char* a = L + row * 128 + 16 * ((2 * (w & 3) + (pp >> 1)) ^ fz) + 8 * (pp & 1);
        wv[ks] = trfrag(a, a + 512);
      }
    } else {
#pragma unroll
      for (int ks = 0; ks < 8; ++ks) asm volatile("" : "=v"(wv[ks]));
    }
    LBAR();
#pragma unroll
    for (int i = 0; i < 8; ++i) PIN(wv[i]);
    v4u wsel[4];
    {
      const v4u wt = *LP(const v4u, L_WT + (cb_b * 32 + kh * 16 + g * 4) * 4);
      wsel[0] = selfrag(wt.x, mask); wsel[1] = selfrag(wt.y, mask); wsel[2] = selfrag(wt.z, mask); wsel[3] = selfrag(wt.w, mask);
#pragma unroll
      for (int i = 0; i < 4; ++i) PIN(wsel[i]);
      SB();
#pragma unroll 1
      for (u32 sp = 0; sp < SPIN_CAP; ++sp) {
        u32 bd = 0;
#pragma unroll
        for (int i = 0; i < 4; ++i) bd |= __any(max4(of[i]) == SENT) ? 1u << i : 0u;
        if (!bd) break;
        CBAR(); SB();
#pragma unroll
        for (int i = 0; i < 4; ++i) if (bd >> i & 1) of[i] = ldh(rw, oo, i * 4 * H * DC * 2);
      }
    }
    {
      f4 acc = {0.f, 0.f, 0.f, 0.f};
#pragma unroll
      for (int i = 0; i < 4; ++i) acc = mfma16(wsel[i], of[i], acc);
      *LP(v2u, a2w) = v2u{pk2(acc.x, acc.y), pk2(acc.z, acc.w)};
    }
    LBAR();
    if (w < 4) {
      v4u af[8];
#pragma unroll
      for (int ks = 0; ks < 8; ++ks) af[ks] = *LP(const v4u, a2r + 64 * ks);
      SB();
      f4 acc = {0.f, 0.f, 0.f, 0.f};
#pragma unroll
      for (int ks = 0; ks < 8; ++ks) acc = mfma16(af[ks], wv[ks], acc);
      const float v = (g & 1) ? acc.z + acc.w : acc.x + acc.y;
      *LP(u16, a3w) = (u16)pk2(v, v);
    }
    LBAR();
    {
      const v4u a0 = *LP(const v4u, a3r), a1 = *LP(const v4u, a3r + 64);
#pragma unroll
      for (int pi = 0; pi < 3; ++pi) {
        if (pi == 2 && w >= 4) break;
        f4 t0 = {0.f, 0.f, 0.f, 0.f}, t1 = {0.f, 0.f, 0.f, 0.f};
        t0 = mfma16(a0, wo[pi * 4 + 0], t0); t1 = mfma16(a0, wo[pi * 4 + 2], t1);
        t0 = mfma16(a1, wo[pi * 4 + 1], t0); t1 = mfma16(a1, wo[pi * 4 + 3], t1);
        sth1(guard(fu((g & 1) ? t1.x : t0.x)), rw, po + pi * 1024);
      }
    }
  } else {
    const int i3 = ((h & 3) == 1 ? 24 + (h >> 2) : (h >> 2) * 3 + ((h & 3) >> 1) + ((h & 3) == 3)) * 2 + j - 2;
    if (i3 < 54) {
      const int pair = i3 >> 1, half = i3 & 1;
      const int pfo = W_PF + i3 * 10240 + (((g >> 1) * 5 + w - 1) * 2 + (g & 1)) * 256 + r * 16;
      if (w == 0) { sth(sent4, rw, W_FX + i3 * 2048 + l * 16); sth(sent4, rw, W_FX + i3 * 2048 + 1024 + l * 16); }
      else if (w < 6) { sth(sent4, rw, pfo); sth(sent4, rw, pfo, 5120); }
      SB();
      u32 xv[5];
#pragma unroll
      for (int f = 0; f < 5; ++f) xv[f] = __builtin_amdgcn_raw_buffer_load_b32(rx, (bsel * M + 320 * half + 40 * w + 10 * g) * 2 + 4 * f, 0, 0);
      v4u wf[10], wdn[8];
      const GAS u16* src = (const GAS u16*)((r >> 3) ? p.wu : p.wg) + (320 * half + 40 * w + 10 * g) * F + 64 * pair + 8 * (r & 7);
      if (w >= 1 && w < 6) {
        const rsrc_t rd = mk(p.wd, F * M * 2);
        const int vo = ((32 * i3 + 8 * g) * M + 128 * (w - 1) + 8 * r) * 2;
#pragma unroll
        for (int f = 0; f < 8; ++f) wdn[f] = ldw(rd, vo, f * M * 2);
      } else {
#pragma unroll
        for (int f = 0; f < 8; ++f) asm volatile("" : "=v"(wdn[f]));
      }
      SB();
#pragma unroll
      for (int f = 0; f < 10; ++f) { wf[f] = __builtin_nontemporal_load((const GAS v4u*)(src + f * F)); SB(); }
      v4u sel[10];
#pragma unroll
      for (int f = 0; f < 5; ++f) { sel[2 * f] = selfrag(duplo(xv[f]), mask); sel[2 * f + 1] = selfrag(duphi(xv[f]), mask); PIN(sel[2 * f]); PIN(sel[2 * f + 1]); }
      SB();
      f4 acc = {0.f, 0.f, 0.f, 0.f};
#pragma unroll
      for (int f = 0; f < 10; ++f) acc = mfma16(sel[f], wf[f], acc);
      *(LAS f4*)(L + (w * 64 + l) * 16) = acc;
      DRAIN();
      LBAR();
      if (w == 0 || w == 7) {
        v4u z0, z1;
        const int zo = W_FX + (i3 ^ 1) * 2048 + l * 16;
        if (w == 7) { z0 = ldh(rw, zo); z1 = ldh(rw, zo + 1024); }
        f4 s = *(const LAS f4*)(L + l * 16);
#pragma unroll
        for (int i = 1; i < 8; ++i) s += *(const LAS f4*)(L + (i * 64 + l) * 16);
        if (w == 0) {
          sth(gran(fu(s.x), fu(s.y), e1), rw, W_FX + i3 * 2048 + l * 16);
          sth(gran(fu(s.z), fu(s.w), e1), rw, W_FX + i3 * 2048 + 1024 + l * 16);
        } else {
          if (__any(stale(z0, e1) | stale(z1, e1))) POLL((z0 = ldh(rw, zo), z1 = ldh(rw, zo + 1024)), __any(stale(z0, e1) | stale(z1, e1)));
          s += f4{uf(z0.x), uf(z0.z), uf(z1.x), uf(z1.z)};
          float hv[4];
#pragma unroll
          for (int i = 0; i < 4; ++i) { const float gs = s[i], us = rorn<8>(s[i]); hv[i] = gs * __builtin_amdgcn_rcpf(1.f + __expf(-gs)) * us; }
          if ((r >> 2) == half) *LP(v2u, 8192 + (g >> 1) * 64 + (8 * (r & 3) + 4 * (g & 1)) * 2) = v2u{pk2(hv[0], hv[1]), pk2(hv[2], hv[3])};
        }
      }
      LBAR();
      if (w >= 1 && w < 6) {
        const v4u z = *LP(const v4u, 8192 + bsel * 64 + g * 16);
        const u32 zz[4] = {z.x, z.y, z.z, z.w};
        acc = f4{0.f, 0.f, 0.f, 0.f};
#pragma unroll
        for (int f = 0; f < 4; ++f) {
          acc = mfma16(selfrag(duplo(zz[f]), mask), wdn[2 * f], acc);
          acc = mfma16(selfrag(duphi(zz[f]), mask), wdn[2 * f + 1], acc);
        }
        sth(gran(fu(acc.x), fu(acc.y), e1), rw, pfo);
        sth(gran(fu(acc.z), fu(acc.w), e1), rw, pfo, 5120);
      }
    }
  }
done:;
}

extern "C" void kernel_launch(void* const* d_in, const int* in_sizes, int n_in,
                              void* d_out, int out_size, void* d_ws, size_t ws_size,
                              hipStream_t stream) {
  Params p;
  p.x = (const u16*)d_in[0]; p.ckv = (const u16*)d_in[1]; p.wq = (const u16*)d_in[2]; p.wuk = (const u16*)d_in[3];
  p.wuv = (const u16*)d_in[4]; p.wo = (const u16*)d_in[5]; p.wg = (const u16*)d_in[7]; p.wu = (const u16*)d_in[8];
  p.wd = (const u16*)d_in[9]; p.gw = (const u16*)d_in[10]; p.out = (u16*)d_out; p.ws = (unsigned char*)d_ws;
  (void)hipFuncSetAttribute((const void*)mk_kernel, hipFuncAttributeMaxDynamicSharedMemorySize, LDS_BYTES);
  mk_kernel<<<256, NT, LDS_BYTES, stream>>>(p);
}
```
